```python
import torch

def _softmax(x, axis=-1):
    return torch.softmax(x, dim=axis)
def _gelu(x):
    return torch.nn.functional.gelu(x, approximate="tanh")
def _silu(x):
    return torch.nn.functional.silu(x)
def _sigmoid(x):
    return torch.sigmoid(x)
def _layernorm(x, axis=-1, eps=1e-5):
    m = x.mean(dim=axis, keepdim=True)
    v = x.var(dim=axis, keepdim=True, unbiased=False)
    return (x - m) / torch.sqrt(v + eps)
def _scatter_add(tgt, idx, src, axis=0):
    return tgt.clone().index_add_(axis, idx, src)
def _segment_sum(src, seg, n):
    out = torch.zeros((n,) + src.shape[1:], dtype=src.dtype)
    return out.index_add_(0, seg, src)
def _pad(x, pw):
    flat = tuple(v for pair in reversed(pw) for v in pair)
    return torch.nn.functional.pad(x, flat)

kernel_name = 'sequential_preLN__mha_block__moe_arch_mod__prompt'

def setup_inputs():

    _w = (lambda a: (lambda t: t.to(torch.bfloat16) if t.is_floating_point() else t)(torch.as_tensor(a)))
    def _A_setup():
        B, S, D, H = 1, 2048, 512, 32
        return {
            "x": _w((lambda *s: torch.randn(*s, dtype=torch.bfloat16) * 0.05)(B, S, D)),
            "wqkv": _w((lambda *s: torch.randn(*s, dtype=torch.bfloat16) * 0.05)(D, 3 * D)),
            "wo": _w((lambda *s: torch.randn(*s, dtype=torch.bfloat16) * 0.05)(D, D)),
        }
    _A_in = _A_setup()

    def _B_setup():
        B, S, D, F, C = 1, 2048, 512, 1792, 1
        return {
            "x": _w((lambda *s: torch.randn(*s, dtype=torch.bfloat16) * 0.05)(B, S, D)),
            "w_r": _w((lambda *s: torch.randn(*s, dtype=torch.bfloat16) * 0.05)(D)),
            "w_gate": _w((lambda *s: torch.randn(*s, dtype=torch.bfloat16) * 0.05)(D, F)),
            "w_up": _w((lambda *s: torch.randn(*s, dtype=torch.bfloat16) * 0.05)(D, F)),
            "w_down": _w((lambda *s: torch.randn(*s, dtype=torch.bfloat16) * 0.05)(F, D)),
        }
    _B_in = _B_setup()

    return {
        "A_x": _A_in["x"],
        "A_wqkv": _A_in["wqkv"],
        "A_wo": _A_in["wo"],
        "B_x": _B_in["x"],
        "B_w_r": _B_in["w_r"],
        "B_w_gate": _B_in["w_gate"],
        "B_w_up": _B_in["w_up"],
        "B_w_down": _B_in["w_down"],
    }

def reference(A_x, A_wqkv, A_wo, B_x, B_w_r, B_w_gate, B_w_up, B_w_down):
    def _A_ref(x, wqkv, wo):
        B, S, D = x.shape
        H = 32
        Dh = D // H
        qkv = (lambda x, *d: x.permute(*d))((x @ wqkv).reshape(B, S, 3, H, Dh), 2, 0, 3, 1, 4)
        q, k, v = qkv[0], qkv[1], qkv[2]
        scores = torch.einsum("bhqd,bhkd->bhqk", q, k) * (Dh ** -0.5)
        p = _softmax(scores, axis=-1)
        o = torch.einsum("bhqk,bhkd->bhqd", p, v)
        return (lambda x, *d: x.permute(*d))(o, 0, 2, 1, 3).reshape(B, S, D) @ wo

    def _B_ref(x, w_r, w_gate, w_up, w_down):
        B, S, D = x.shape
        C = 1
        r = x @ w_r
        idx = torch.argsort(-r, axis=-1)[:, :C]
        idx_e = (lambda x, n, axis: torch.repeat_interleave(x, n, dim=axis))(idx.reshape(B, C, 1), D, axis=2)
        x_sel = (lambda x, i, axis: torch.take_along_dim(x, i, dim=axis))(x, idx_e, axis=1)
        g = _silu(x_sel @ w_gate)
        u = x_sel @ w_up
        y = (g * u) @ w_down
        r_sel = _sigmoid((lambda x, i, axis: torch.take_along_dim(x, i, dim=axis))(r, idx, axis=1))
        y = y * r_sel.reshape(B, C, 1)
        b_off = (torch.arange(B) * S).reshape(B, 1)
        flat_idx = (idx + b_off).reshape(B * C)
        zero = (x * 0.0).reshape(B * S, D)
        upd = _scatter_add(zero, flat_idx, y.reshape(B * C, D), axis=0)
        return x + upd.reshape(B, S, D)

    x = A_x
    h = x + _A_ref(_layernorm(x, axis=-1), A_wqkv, A_wo)
    h2 = h + _B_ref(_layernorm(h, axis=-1), B_w_r, B_w_gate, B_w_up, B_w_down)
    return h2
```

```cpp
#include <hip/hip_runtime.h>
#include <hip/hip_bf16.h>
#include <type_traits>

namespace {

typedef unsigned u32;
typedef unsigned long long u64;
typedef unsigned short u16;
typedef u32 u32x2 __attribute__((ext_vector_type(2)));
typedef u32 u32x4 __attribute__((ext_vector_type(4)));
typedef float f32x4 __attribute__((ext_vector_type(4)));
typedef float f32x8 __attribute__((ext_vector_type(8)));
typedef float f32x16 __attribute__((ext_vector_type(16)));
typedef __bf16 bf16x2 __attribute__((ext_vector_type(2)));
typedef __bf16 bf16x8 __attribute__((ext_vector_type(8)));
typedef __bf16 bf16x16 __attribute__((ext_vector_type(16)));
typedef short s16x4 __attribute__((ext_vector_type(4)));
#define LAS __attribute__((address_space(3)))
#define GAS __attribute__((address_space(1)))
#define DEV __device__ __forceinline__

constexpr int DM = 512, FF = 1792;
constexpr int NWG = 256, NT = 1024;
constexpr int N_GU = 224;

constexpr u32 WS_EPOCH = 0;
constexpr u32 WS_FLAG = 4096;
constexpr u32 WS_BEST = 8192;
constexpr u32 WS_ACT = 12288;
constexpr u32 WS_ST = 16384;
constexpr u32 WS_ROWN = WS_ST + 64 * 4 * 512;
constexpr u32 WS_ROWH = WS_ROWN + 64 * 1024;
constexpr u32 WS_KV = 1u << 20;
constexpr u32 WS_OB = WS_KV + 256 * 16384;
constexpr u32 WS_DBG = WS_OB + 64 * 32 * 1024;
constexpr u32 WS_BYTES = WS_DBG + 65536;

constexpr u32 L_K = 0;
constexpr u32 VSTR = 1040;
constexpr u32 L_V = 65536;
constexpr u32 L_S = L_V + 64 * VSTR;
constexpr u32 L_WQ = L_K + 4 * 1024, L_WK = L_K + 36 * 1024, L_WV = L_S;
constexpr u32 L_QX = L_S + 16384;
constexpr u32 L_PRIV0 = L_S;
constexpr u32 L_PRIV1 = L_S + 8192;
constexpr u32 L_MISC = L_S + 28672;
constexpr u32 L_CS = L_MISC;
constexpr u32 L_CNT = L_MISC + 256;
constexpr u32 L_WIN = L_MISC + 272;
constexpr u32 L_TOK = L_MISC + 512;
constexpr u32 L_BT = L_MISC + 768;
constexpr u32 L_RED = L_MISC + 1024;
constexpr u32 L_BYTES = 163840;
static_assert(L_MISC + 2048 + 1024 <= L_BYTES, "lds");
constexpr u32 L_OI = 0;
constexpr u32 L_XC = 32768;
constexpr u32 L_PS = 49152;

constexpr u32 SENT = 0xFFFFFFFFu;
constexpr int AUX_SC1 = 16;
constexpr u32 SPIN_CAP = 1u << 17;

struct Params {
  const u16* x;
  const u16* wqkv;
  const u16* wo;
  const u16* wr;
  const u16* wg;
  const u16* wu;
  const u16* wd;
  u16* out;
  unsigned char* ws;
};

DEV u32 pack2(float a, float b) {
  bf16x2 v = {(__bf16)a, (__bf16)b};
  return __builtin_bit_cast(u32, v);
}
DEV float asf(u32 v) { return __builtin_bit_cast(float, v); }
DEV float lo16(u32 v) { return __builtin_bit_cast(float, v << 16); }
DEV float hi16(u32 v) { return __builtin_bit_cast(float, v & 0xFFFF0000u); }
DEV float rbf(float a) { return (float)(__bf16)a; }
DEV u32 clampw(u32 v) { return v < 0xFFFFFFFEu ? v : 0xFFFFFFFEu; }
DEV u32x4 clampw4(u32x4 v) { return u32x4{clampw(v.x), clampw(v.y), clampw(v.z), clampw(v.w)}; }
DEV bool has_sent(u32x4 v) { return (v.x == SENT) | (v.y == SENT) | (v.z == SENT) | (v.w == SENT); }
DEV f32x4 mfma16(u32x4 a, u32x4 b, f32x4 c) {
  return __builtin_amdgcn_mfma_f32_16x16x32_bf16(__builtin_bit_cast(bf16x8, a), __builtin_bit_cast(bf16x8, b), c, 0, 0, 0);
}
DEV f32x16 mfma32(u32x4 a, u32x4 b, f32x16 c) {
  return __builtin_amdgcn_mfma_f32_32x32x16_bf16(__builtin_bit_cast(bf16x8, a), __builtin_bit_cast(bf16x8, b), c, 0, 0, 0);
}
DEV f32x16 smfmac(u32x4 a, f32x8 b, f32x16 c) {
  return __builtin_amdgcn_smfmac_f32_32x32x32_bf16(__builtin_bit_cast(bf16x8, a), __builtin_bit_cast(bf16x16, b), c,
                                                   (int)0xDDDDDDDD, 0, 0);
}
DEV float dot2(u32 a, u32 b, float c) {
  return __builtin_amdgcn_fdot2_f32_bf16(__builtin_bit_cast(bf16x2, a), __builtin_bit_cast(bf16x2, b), c, false);
}
DEV u32x4 lds128(LAS unsigned char* L, u32 off) { return *(LAS u32x4*)(L + off); }
DEV void sts128(LAS unsigned char* L, u32 off, u32x4 v) { *(LAS u32x4*)(L + off) = v; }
DEV u32x2 ldtr(LAS unsigned char* L, u32 off) {
  return __builtin_bit_cast(u32x2, __builtin_amdgcn_ds_read_tr16_b64_v4i16((LAS s16x4*)(L + off)));
}
DEV u32x4 ldtr2(LAS unsigned char* L, u32 off0, u32 off1) {
  u32x2 a = ldtr(L, off0), b = ldtr(L, off1);
  return u32x4{a.x, a.y, b.x, b.y};
}
DEV u32x4 gld128(const u16* p) { return *(const GAS u32x4*)p; }
DEV u32x2 gld64(const u16* p) { return *(const GAS u32x2*)p; }
DEV float add16(float v) {
  u32 u = __builtin_bit_cast(u32, v);
  auto r = __builtin_amdgcn_permlane16_swap(u, u, false, false);
  return __builtin_bit_cast(float, (u32)r[0]) + __builtin_bit_cast(float, (u32)r[1]);
}
DEV float add32(float v) {
  u32 u = __builtin_bit_cast(u32, v);
  auto r = __builtin_amdgcn_permlane32_swap(u, u, false, false);
  return __builtin_bit_cast(float, (u32)r[0]) + __builtin_bit_cast(float, (u32)r[1]);
}
DEV float max32(float v) {
  u32 u = __builtin_bit_cast(u32, v);
  auto r = __builtin_amdgcn_permlane32_swap(u, u, false, false);
  return fmaxf(__builtin_bit_cast(float, (u32)r[0]), __builtin_bit_cast(float, (u32)r[1]));
}
DEV float max16x(f32x16 s) {
  float a = fmaxf(fmaxf(s[0], s[1]), s[2]), b = fmaxf(fmaxf(s[3], s[4]), s[5]), c = fmaxf(fmaxf(s[6], s[7]), s[8]);
  float d = fmaxf(fmaxf(s[9], s[10]), s[11]), e = fmaxf(fmaxf(s[12], s[13]), s[14]);
  return fmaxf(fmaxf(fmaxf(a, b), c), fmaxf(fmaxf(d, e), s[15]));
}
DEV u64 umax64(u64 a, u64 b) { return a > b ? a : b; }
DEV u64 wave_max64(u64 k) {
#pragma unroll
  for (int m = 32; m >= 1; m >>= 1) {
    u32 lo = __shfl_xor((u32)k, m), hi = __shfl_xor((u32)(k >> 32), m);
    k = umax64(k, ((u64)hi << 32) | lo);
  }
  return k;
}
#define DRAIN() asm volatile("s_waitcnt vmcnt(0)" ::: "memory")
#define ORDER() __builtin_amdgcn_fence(__ATOMIC_ACQUIRE, "wavefront")
DEV void lds_barrier() {
  asm volatile("s_waitcnt lgkmcnt(0)" ::: "memory");
  __builtin_amdgcn_s_barrier();
  asm volatile("" ::: "memory");
}

#define STAMP(i)

__global__ void __launch_bounds__(NT) fused_block_kernel(Params p) {
  extern __shared__ __attribute__((aligned(16))) unsigned char smem[];
  LAS unsigned char* L = (LAS unsigned char*)smem;
  const int tid = threadIdx.x;
  int lane = tid & 63;
  const int w = __builtin_amdgcn_readfirstlane(tid >> 6);
  const int bid = blockIdx.x;
  const int xg = bid & 7, xi = bid >> 3;
  const int h = 8 * (xg >> 1) + (xi & 7), b = 4 * (xg & 1) + (xi >> 3);
  int l15 = lane & 15, g = lane >> 4, l31 = lane & 31, hh = lane >> 5;
#define RELANE()                                                                        \
  do {                                                                                  \
    lane = __builtin_amdgcn_mbcnt_hi(~0u, __builtin_amdgcn_mbcnt_lo(~0u, 0u));          \
    asm volatile("" : "+v"(lane));                                                      \
    l15 = lane & 15; g = lane >> 4; l31 = lane & 31; hh = lane >> 5;                    \
  } while (0)
  const __amdgpu_buffer_rsrc_t rs = __builtin_amdgcn_make_buffer_rsrc(p.ws, 0, WS_BYTES, 0x00020000);
  const int panel = 8 * b + (h >> 2), cq = h & 3;
  STAMP(0);

  u32x4 wst[3];
  {
    const u16* src = p.wqkv + (size_t)(tid >> 1) * (3 * DM) + h * 16 + (tid & 1) * 8;
#pragma unroll
    for (int c = 0; c < 3; ++c) wst[c] = gld128(src + c * DM);
  }
  u32x4 xf[16];
  {
    const u16* src = p.x + (size_t)(256 * b + 16 * w + l15) * DM + g * 8;
#pragma unroll
    for (int ks = 0; ks < 16; ++ks) xf[ks] = gld128(src + ks * 32);
  }
  const u64 epoch = __hip_atomic_load((GAS u64*)(p.ws + WS_EPOCH), __ATOMIC_RELAXED, __HIP_MEMORY_SCOPE_AGENT);
  {
    const u32x4 s4 = {SENT, SENT, SENT, SENT};
    if (w < 8) {
      __builtin_amdgcn_raw_buffer_store_b128(s4, rs, WS_OB + ((8 * b + w) * 32 + h) * 1024 + lane * 16, 0, AUX_SC1);
    } else if (w == 8) {
      u32 off = lane < 32 ? WS_ST + (panel * 4 + cq) * 512 + lane * 16
                          : (lane < 48 ? WS_ROWN : WS_ROWH) + panel * 1024 + cq * 256 + l15 * 16;
      __builtin_amdgcn_raw_buffer_store_b128(s4, rs, off, 0, AUX_SC1);
    } else if (w == 9) {
      if (lane == 0 && cq == 0) __builtin_amdgcn_raw_buffer_store_b64(u32x2{SENT, SENT}, rs, WS_BEST + panel * 8, 0, AUX_SC1);
      if (lane == 1 && bid < N_GU) __builtin_amdgcn_raw_buffer_store_b128(s4, rs, WS_ACT + bid * 16, 0, AUX_SC1);
    } else if (w == 10) {
      sts128(L, L_V + lane * VSTR + 1024, u32x4{0x3F803F80u, 0x3F803F80u, 0x3F803F80u, 0x3F803F80u});
    } else if (w == 11) {
      if (lane == 0) *(LAS u32*)(L + L_CNT) = 0;
    }
  }
  {
    const int k = tid >> 1;
    const u32 pos = (k & ~31) + ((k & 4) << 2) + ((k >> 1) & 12) + (k & 3);
    const u32 off = pos * 32 + (tid & 1) * 16;
    sts128(L, L_WQ + off, wst[0]);
    sts128(L, L_WK + off, wst[1]);
    sts128(L, L_WV + off, wst[2]);
  }
  lds_barrier();
  STAMP(1);
  const u32 wtr = lane * 8;
  const u32x4 ones4 = {0x3F803F80u, 0x3F803F80u, 0x3F803F80u, 0x3F803F80u};
  if (w < 3) {
    const u32 base = (w == 0 ? L_WQ : (w == 1 ? L_WK : L_WV)) + wtr;
    f32x4 cs = {0.f, 0.f, 0.f, 0.f};
#pragma unroll
    for (int ks = 0; ks < 16; ++ks) cs = mfma16(ldtr2(L, base + ks * 1024, base + ks * 1024 + 512), ones4, cs);
    if (l15 == 0) *(LAS f32x4*)(L + L_CS + w * 64 + g * 16) = cs;
  }
  f32x4 aq = {0.f, 0.f, 0.f, 0.f}, ak = aq, av = aq;
  float s1 = 0.f, s2 = 0.f;
#pragma unroll
  for (int ks = 0; ks < 16; ++ks) {
    const u32x4 wq = ldtr2(L, L_WQ + wtr + ks * 1024, L_WQ + wtr + ks * 1024 + 512);
    const u32x4 wk = ldtr2(L, L_WK + wtr + ks * 1024, L_WK + wtr + ks * 1024 + 512);
    const u32x4 wv = ldtr2(L, L_WV + wtr + ks * 1024, L_WV + wtr + ks * 1024 + 512);
    aq = mfma16(wq, xf[ks], aq);
    ak = mfma16(wk, xf[ks], ak);
    av = mfma16(xf[ks], wv, av);
#pragma unroll
    for (int i = 0; i < 4; ++i) {
      s1 = dot2(xf[ks][i], 0x3F803F80u, s1);
      s2 = dot2(xf[ks][i], xf[ks][i], s2);
    }
    if (ks == 7) lds_barrier();
  }
  {
    s1 = add32(add16(s1));
    s2 = add32(add16(s2));
    const float mean = s1 * (1.f / DM);
    const float var = fmaxf(s2 * (1.f / DM) - mean * mean, 0.f);
    const float mb = rbf(mean);
    const float rd = 1.f / rbf(__builtin_sqrtf(rbf(rbf(var) + 1e-5f)));
    const f32x4 csq = *(LAS f32x4*)(L + L_CS + g * 16);
    const f32x4 csk = *(LAS f32x4*)(L + L_CS + 64 + g * 16);
    const float csv = *(LAS float*)(L + L_CS + 128 + l15 * 4);
    const float rq = rd * (0.25f * 1.44269504088896340736f);
    float qv[4], kv[4], vv[4];
#pragma unroll
    for (int i = 0; i < 4; ++i) {
      qv[i] = (aq[i] - mb * csq[i]) * rq;
      kv[i] = (ak[i] - mb * csk[i]) * rd;
      const float mt = __shfl(mb, 4 * g + i), rt = __shfl(rd, 4 * g + i);
      vv[i] = (av[i] - mt * csv) * rt;
    }
    const int u = w >> 1, sh = w & 1;
    *(LAS u32x2*)(L + L_QX + u * 1024 + (g >> 1) * 512 + (16 * sh + l15) * 16 + (g & 1) * 8) =
        u32x2{pack2(qv[0], qv[1]), pack2(qv[2], qv[3])};
    auto r0 = __builtin_amdgcn_permlane16_swap(pack2(kv[0], kv[1]), pack2(vv[0], vv[1]), false, false);
    auto r1 = __builtin_amdgcn_permlane16_swap(pack2(kv[2], kv[3]), pack2(vv[2], vv[3]), false, false);
    const u32x4 kvw = {(u32)r0[0], (u32)r1[0], (u32)r0[1], (u32)r1[1]};
    const u32 inner = (g & 1) ? sh * 512 + hh * 256 + l15 * 16 : hh * 512 + (16 * sh + l15) * 16;
    __builtin_amdgcn_raw_buffer_store_b128(kvw, rs, WS_KV + (h * 8 + b) * 16384 + (g & 1) * 8192 + u * 1024 + inner, 0, AUX_SC1);
    const int pos = 32 * (u >> 2) + (u & 3);
    sts128(L, ((g & 1) ? L_V + pos * VSTR : L_K + pos * 1024) + inner, kvw);
  }
  lds_barrier();
  STAMP(2);

  const u64 tag = epoch * 6364136223846793005ull + 1442695040888963407ull;
  const int qt = w & 7, kh = w >> 3;
  const u32x4 qf = lds128(L, L_QX + qt * 1024 + lane * 16);
  const u32 ka = L_K + kh * 32 * 1024 + lane * 16;
  const u32 va = L_V + kh * 32 * VSTR + ((lane & 16) ? 1024 - 0 : hh * 256 + l15 * 16);
  const u32 va1 = L_V + kh * 32 * VSTR + ((lane & 16) ? 1024 - 512 : hh * 256 + l15 * 16);
  f32x16 negm;
  {
    const f32x16 z = {};
    const float m = max32(max16x(mfma32(lds128(L, L_K + lane * 16), qf, z)));
#pragma unroll
    for (int i = 0; i < 16; ++i) negm[i] = -m;
  }
  DRAIN();
  if (lane == 0) {
    const u32 old = __hip_atomic_fetch_add((LAS u32*)(L + L_CNT), 1u, __ATOMIC_RELAXED, __HIP_MEMORY_SCOPE_WORKGROUP);
    if (old == 15) __hip_atomic_store((GAS u64*)(p.ws + WS_FLAG) + h * 8 + b, tag, __ATOMIC_RELAXED, __HIP_MEMORY_SCOPE_AGENT);
  }
  f32x16 o = {};
  auto tiles = [&](auto nc, u32 k0, u32 v0a, u32 v1a) {
    constexpr int N = decltype(nc)::value;
    asm volatile("" : "+v"(k0), "+v"(v0a), "+v"(v1a));
#pragma unroll
    for (int t = 0; t < N; ++t) {
      const u32x4 kf = lds128(L, k0 + t * 1024);
      const u32x4 v0 = lds128(L, v0a + t * VSTR);
      const u32x4 v1 = lds128(L, v1a + t * VSTR);
      f32x16 s = mfma32(kf, qf, negm);
      f32x8 p0, p1;
#pragma unroll
      for (int i = 0; i < 8; ++i) p0[i] = __builtin_amdgcn_exp2f(s[i]);
      o = smfmac(v0, p0, o);
#pragma unroll
      for (int i = 0; i < 8; ++i) p1[i] = __builtin_amdgcn_exp2f(s[8 + i]);
      o = smfmac(v1, p1, o);
    }
  };
  using I2 = std::integral_constant<int, 2>;
  using I4 = std::integral_constant<int, 4>;
  tiles(I2{}, ka, va, va1 + 512);
  {
    GAS u64* fl = (GAS u64*)(p.ws + WS_FLAG) + h * 8 + (lane & 7);
    for (u32 spin = 0; spin < SPIN_CAP; ++spin) {
      const u64 f = __hip_atomic_load(fl, __ATOMIC_RELAXED, __HIP_MEMORY_SCOPE_AGENT);
      if (__all(f == tag)) break;
      __builtin_amdgcn_s_sleep(2);
    }
    ORDER();
  }
  STAMP(3);
  u32x4 stg[8];
#pragma unroll
  for (int j = 1; j < 8; ++j)
    stg[j] = __builtin_amdgcn_raw_buffer_load_b128(rs, WS_KV + (h * 8 + ((b + j) & 7)) * 16384 + w * 1024 + lane * 16, 0, AUX_SC1);
  tiles(I2{}, ka + 2 * 1024, va + 2 * VSTR, va1 + 512 + 2 * VSTR);
  {
    const int u = w & 7, pos = 32 * (u >> 2) + (u & 3);
    const u32 str = kh ? VSTR : 1024;
    const u32 base = (kh ? L_V : L_K) + pos * str + lane * 16;
#pragma unroll
    for (int j = 1; j < 8; ++j) sts128(L, base + 4 * j * str, stg[j]);
  }
  lds_barrier();
  STAMP(4);
  const int ct = w & 7;
  {
    const u16* src = p.wo + (size_t)(256 * kh + (lane >> 1)) * DM + 128 * cq + 16 * ct + (lane & 1) * 8;
#pragma unroll
    for (int i = 0; i < 8; ++i) stg[i] = gld128(src + (size_t)32 * i * DM);
  }
  const size_t fstr = bid < N_GU ? 0 : 32 * DM;
  auto ffn_src = [&]() -> const u16* {
    if (bid < N_GU) return ((lane & 1) ? p.wu : p.wg) + (size_t)(32 * w + (lane >> 1)) * FF + 8 * bid;
    return p.wd + (size_t)(128 * (w < 14 ? w : 13) + (lane >> 1)) * DM + 16 * (bid - N_GU) + (lane & 1) * 8;
  };
  u32 warm[4];
#pragma unroll
  for (int i = 0; i < 4; ++i) warm[i] = *(const GAS u32*)(ffn_src() + i * fstr);
#pragma unroll 1
  for (int t = 4; t < 12; t += 4) tiles(I4{}, ka + t * 1024, va + t * VSTR, va1 + 512 + t * VSTR);
  RELANE();
  const u32 priv = kh ? L_PRIV1 + qt * 2304 : L_PRIV0 + qt * 1024;
  {
    const u32 q4 = (lane >> 2) & 3, pp = lane & 3;
    const u32 a_wo = priv + (16 * (g >> 1) + 4 * (g & 1) + q4) * 32 + pp * 8;
#pragma unroll
    for (int i = 0; i < 8; ++i) {
      sts128(L, priv + lane * 16, stg[i]);
      stg[i] = ldtr2(L, a_wo, a_wo + 256);
    }
    asm volatile("" ::"v"(warm[0]), "v"(warm[1]), "v"(warm[2]), "v"(warm[3]));
  }
  if (w == 0) {
    for (u32 spin = 0; spin < SPIN_CAP; ++spin) {
      const u32x4 f0 = __builtin_amdgcn_raw_buffer_load_b128(rs, WS_FLAG + lane * 16, 0, AUX_SC1);
      const u32x4 f1 = __builtin_amdgcn_raw_buffer_load_b128(rs, WS_FLAG + 1024 + lane * 16, 0, AUX_SC1);
      const u32 tl = (u32)tag, th = (u32)(tag >> 32);
      const bool ok = (f0.x == tl) & (f0.y == th) & (f0.z == tl) & (f0.w == th) & (f1.x == tl) & (f1.y == th) & (f1.z == tl) & (f1.w == th);
      if (__all(ok)) break;
      __builtin_amdgcn_s_sleep(2);
    }
    ORDER();
    if (bid == 0 && lane == 0) __hip_atomic_store((GAS u64*)(p.ws + WS_EPOCH), tag, __ATOMIC_RELAXED, __HIP_MEMORY_SCOPE_AGENT);
  }
#pragma unroll 1
  for (int t = 12; t < 32; t += 4) tiles(I4{}, ka + t * 1024, va + t * VSTR, va1 + 512 + t * VSTR);
  RELANE();
  if (kh) {
    *(LAS f32x4*)(L + priv + lane * 16) = f32x4{o[0], o[1], o[2], o[3]};
    *(LAS f32x4*)(L + priv + 1024 + lane * 16) = f32x4{o[4], o[5], o[6], o[7]};
    *(LAS float*)(L + priv + 2048 + lane * 4) = o[8];
  }
  lds_barrier();
  STAMP(5);
  if (!kh) {
    const u32 pb = L_PRIV1 + qt * 2304;
    const f32x4 a = *(LAS f32x4*)(L + pb + lane * 16), c = *(LAS f32x4*)(L + pb + 1024 + lane * 16);
    o[8] += *(LAS float*)(L + pb + 2048 + lane * 4);
    float amax = 0.f;
#pragma unroll
    for (int i = 0; i < 4; ++i) {
      o[i] += a[i];
      o[4 + i] += c[i];
      amax = fmaxf(amax, fmaxf(fabsf(o[i]), fabsf(o[4 + i])));
    }
    const bool safe = (o[8] < 1.0995116e12f) & (amax < 3.0e38f);
    if (!__all(safe)) {
      const f32x16 z = {};
      float m = -INFINITY;
#pragma unroll 1
      for (int t = 0; t < 64; ++t) m = fmaxf(m, max16x(mfma32(lds128(L, L_K + t * 1024 + lane * 16), qf, z)));
      m = max32(m);
#pragma unroll
      for (int i = 0; i < 16; ++i) { negm[i] = -m; o[i] = 0.f; }
#pragma unroll 1
      for (int t = 0; t < 64; t += 2) tiles(I2{}, ka + t * 1024, va + t * VSTR, va1 + 512 + t * VSTR);
    }
    const float rl = 1.f / o[8];
    const u32x4 ow = {pack2(o[0] * rl, o[1] * rl), pack2(o[2] * rl, o[3] * rl), pack2(o[4] * rl, o[5] * rl), pack2(o[6] * rl, o[7] * rl)};
    __builtin_amdgcn_raw_buffer_store_b128(clampw4(ow), rs, WS_OB + ((8 * b + qt) * 32 + h) * 1024 + lane * 16, 0, AUX_SC1);
  }

  RELANE();
  u32x4 ff[4];
#pragma unroll
  for (int i = 0; i < 4; ++i) ff[i] = gld128(ffn_src() + i * fstr);
  const int tok = 32 * panel + 16 * kh + l15, col = 128 * cq + 16 * ct + 4 * g;
  const u32x2 xres = gld64(p.x + (size_t)tok * DM + col);
  const u32x2 wr2 = gld64(p.wr + col);
  {
    u32x4 o0, o1;
    const u32 off = WS_OB + (panel * 32 + 2 * w) * 1024 + lane * 16;
    for (u32 spin = 0; spin < SPIN_CAP; ++spin) {
      o0 = __builtin_amdgcn_raw_buffer_load_b128(rs, off, 0, AUX_SC1);
      o1 = __builtin_amdgcn_raw_buffer_load_b128(rs, off + 1024, 0, AUX_SC1);
      if (!__any(has_sent(o0) | has_sent(o1))) break;
      __builtin_amdgcn_s_sleep(1);
    }
    lds_barrier();
    sts128(L, L_OI + 2 * w * 1024 + lane * 16, o0);
    sts128(L, L_OI + 2 * w * 1024 + 1024 + lane * 16, o1);
  }
  lds_barrier();
  STAMP(6);
  f32x4 acc;
  {
    f32x4 a0 = {0.f, 0.f, 0.f, 0.f}, a1 = a0;
    const u32 ob = L_OI + (16 * kh + (g >> 1)) * 1024 + ((g & 1) * 32 + l15) * 16;
#pragma unroll
    for (int i = 0; i < 8; ++i) {
      a0 = mfma16(stg[i], lds128(L, ob + i * 2048), a0);
      a1 = mfma16(stg[i], lds128(L, ob + i * 2048 + 256), a1);
    }
    *(LAS f32x4*)(L + L_XC + (w ^ 8) * 1024 + lane * 16) = kh ? a0 : a1;
    acc = kh ? a1 : a0;
  }
  lds_barrier();
  float hv[4];
  {
    const f32x4 oth = *(LAS f32x4*)(L + L_XC + w * 1024 + lane * 16);
    const float xs[4] = {lo16(xres.x), hi16(xres.x), lo16(xres.y), hi16(xres.y)};
    const float ws4[4] = {lo16(wr2.x), hi16(wr2.x), lo16(wr2.y), hi16(wr2.y)};
    float t1 = 0.f, t2 = 0.f, t3 = 0.f, t4 = 0.f;
#pragma unroll
    for (int i = 0; i < 4; ++i) {
      hv[i] = rbf(xs[i] + rbf(acc[i] + oth[i]));
      t1 += hv[i];
      t2 += hv[i] * hv[i];
      t3 += hv[i] * ws4[i];
      t4 += ws4[i];
    }
    t1 = add32(add16(t1));
    t2 = add32(add16(t2));
    t3 = add32(add16(t3));
    t4 = add32(add16(t4));
    if (g == 0) *(LAS f32x4*)(L + L_PS + ((kh * 8 + ct) * 16 + l15) * 16) = f32x4{t1, t2, t3, t4};
  }
  lds_barrier();
  STAMP(7);
  {
    const u32 a_ff = priv + (8 * g + ((lane >> 2) & 3)) * 32 + (lane & 3) * 8;
#pragma unroll
    for (int i = 0; i < 4; ++i) {
      sts128(L, priv + lane * 16, ff[i]);
      ff[i] = ldtr2(L, a_ff, a_ff + 128);
    }
  }
  if (w == 0) {
    f32x4 t = {0.f, 0.f, 0.f, 0.f};
    const u32 pa = L_PS + ((l31 >> 4) * 8 * 16 + l15) * 16;
#pragma unroll
    for (int i = 0; i < 8; ++i) t += *(LAS f32x4*)(L + pa + i * 256);
    if (lane < 32)
      __builtin_amdgcn_raw_buffer_store_b128(clampw4(__builtin_bit_cast(u32x4, t)), rs, WS_ST + (panel * 4 + cq) * 512 + lane * 16, 0, AUX_SC1);
    u32x4 q0, q1;
    const u32 off = WS_ST + panel * 2048 + lane * 16;
    for (u32 spin = 0; spin < SPIN_CAP; ++spin) {
      q0 = __builtin_amdgcn_raw_buffer_load_b128(rs, off, 0, AUX_SC1);
      q1 = __builtin_amdgcn_raw_buffer_load_b128(rs, off + 1024, 0, AUX_SC1);
      if (!__any(has_sent(q0) | has_sent(q1))) break;
      __builtin_amdgcn_s_sleep(1);
    }
    const f32x4 u4 = __builtin_bit_cast(f32x4, q0) + __builtin_bit_cast(f32x4, q1);
    const float a1 = add32(u4[0]), a2 = add32(u4[1]), a3 = add32(u4[2]), a4 = add32(u4[3]);
    const float mean = a1 * (1.f / DM);
    const float var = fmaxf(a2 * (1.f / DM) - mean * mean, 0.f);
    const float mb = rbf(mean);
    const float rd = 1.f / rbf(__builtin_sqrtf(rbf(rbf(var) + 1e-5f)));
    const float r = (a3 - mb * a4) * rd;
    u32 rb = __builtin_bit_cast(u32, r);
    rb = clampw((rb & 0x80000000u) ? ~rb : (rb | 0x80000000u));
    const u64 key = wave_max64(((u64)rb << 32) | (u32)(4095 - (32 * panel + l31)));
    if (lane == 0 && cq == 0)
      __builtin_amdgcn_raw_buffer_store_b64(u32x2{(u32)key, (u32)(key >> 32)}, rs, WS_BEST + panel * 8, 0, AUX_SC1);
    if (lane < 32) *(LAS u32x2*)(L + L_TOK + lane * 8) = u32x2{__builtin_bit_cast(u32, mb), __builtin_bit_cast(u32, rd)};
    if (lane == 0) *(LAS u32*)(L + L_BT) = 4095 - (u32)key;
  }
  lds_barrier();
  STAMP(8);
  u32x2 ow;
  bool held;
  {
    const u32x2 st = *(LAS u32x2*)(L + L_TOK + (16 * kh + l15) * 8);
    const float mb = asf(st.x), rd = asf(st.y);
    float nv[4];
#pragma unroll
    for (int i = 0; i < 4; ++i) nv[i] = rbf(rbf(hv[i] - mb) * rd);
    ow = u32x2{pack2(hv[0] + nv[0], hv[1] + nv[1]), pack2(hv[2] + nv[2], hv[3] + nv[3])};
    held = (u32)tok == *(LAS u32*)(L + L_BT);
    if (held) {
      __builtin_amdgcn_raw_buffer_store_b64(u32x2{clampw(pack2(nv[0], nv[1])), clampw(pack2(nv[2], nv[3]))}, rs,
                                            WS_ROWN + panel * 1024 + col * 2, 0, AUX_SC1);
      __builtin_amdgcn_raw_buffer_store_b64(u32x2{clampw(pack2(hv[0], hv[1])), clampw(pack2(hv[2], hv[3]))}, rs,
                                            WS_ROWH + panel * 1024 + col * 2, 0, AUX_SC1);
    } else {
      *(GAS u32x2*)(p.out + (size_t)tok * DM + col) = ow;
    }
  }
  if (w == 0) {
    u32x2 k2;
    for (u32 spin = 0; spin < SPIN_CAP; ++spin) {
      k2 = __builtin_amdgcn_raw_buffer_load_b64(rs, WS_BEST + lane * 8, 0, AUX_SC1);
      if (!__any((k2.x == SENT) | (k2.y == SENT))) break;
      __builtin_amdgcn_s_sleep(1);
    }
    const u64 key = wave_max64(((u64)k2.y << 32) | k2.x);
    if (lane == 0) *(LAS u32x2*)(L + L_WIN) = u32x2{(4095 - (u32)key) & 2047u, (u32)(key >> 32)};
  }
  lds_barrier();
  STAMP(9);

  const u32x2 win = *(LAS u32x2*)(L + L_WIN);
  const u32 wtok = win.x, wpan = wtok >> 5;
  if (held && (u32)tok != wtok) *(GAS u32x2*)(p.out + (size_t)tok * DM + col) = ow;
  if (bid < N_GU) {
    u32x4 hn;
    for (u32 spin = 0; spin < SPIN_CAP; ++spin) {
      hn = __builtin_amdgcn_raw_buffer_load_b128(rs, WS_ROWN + wpan * 1024 + w * 64 + g * 16, 0, AUX_SC1);
      if (!__any(has_sent(hn))) break;
      __builtin_amdgcn_s_sleep(1);
    }
    const f32x4 d = mfma16(ff[0], hn, f32x4{0.f, 0.f, 0.f, 0.f});
    if (l15 == 0) *(LAS f32x4*)(L + L_RED + w * 64 + g * 16) = d;
    lds_barrier();
    STAMP(10);
    if (w == 0) {
      float v = 0.f;
#pragma unroll
      for (int i = 0; i < 4; ++i) v += *(LAS float*)(L + L_RED + (4 * g + i) * 64 + l15 * 4);
      v = rbf(add32(add16(v)));
      const float up = __shfl(v, (lane & 7) + 8);
      const float gt = rbf(v / (1.f + __expf(-v)));
      const float a = rbf(gt * up);
      const float an = __shfl(a, (lane & 7) | 1);
      if (lane < 8 && !(lane & 1))
        __builtin_amdgcn_raw_buffer_store_b32(clampw(pack2(a, an)), rs, WS_ACT + bid * 16 + lane * 2, 0, AUX_SC1);
    }
  } else {
    const int c0 = 16 * (bid - N_GU);
    u32 hw = 0, nw = 0;
    if (w == 0) {
      for (u32 spin = 0; spin < SPIN_CAP; ++spin) {
        hw = __builtin_amdgcn_raw_buffer_load_b32(rs, WS_ROWH + wpan * 1024 + c0 * 2 + (l15 >> 1) * 4, 0, AUX_SC1);
        nw = __builtin_amdgcn_raw_buffer_load_b32(rs, WS_ROWN + wpan * 1024 + c0 * 2 + (l15 >> 1) * 4, 0, AUX_SC1);
        if (!__any((hw == SENT) | (nw == SENT))) break;
        __builtin_amdgcn_s_sleep(1);
      }
    }
    f32x4 d = {0.f, 0.f, 0.f, 0.f};
    if (w < 14) {
      u32x4 av4[4];
      for (u32 spin = 0; spin < SPIN_CAP; ++spin) {
        bool miss = false;
#pragma unroll
        for (int i = 0; i < 4; ++i) {
          av4[i] = __builtin_amdgcn_raw_buffer_load_b128(rs, WS_ACT + w * 256 + i * 64 + g * 16, 0, AUX_SC1);
          miss |= has_sent(av4[i]);
        }
        if (!__any(miss)) break;
        __builtin_amdgcn_s_sleep(1);
      }
#pragma unroll
      for (int i = 0; i < 4; ++i) d = mfma16(ff[i], av4[i], d);
    }
    if (l15 == 0) *(LAS f32x4*)(L + L_RED + w * 64 + g * 16) = d;
    lds_barrier();
    STAMP(10);
    if (w == 0) {
      float v = 0.f;
#pragma unroll
      for (int i = 0; i < 4; ++i) v += *(LAS float*)(L + L_RED + (4 * g + i) * 64 + l15 * 4);
      v = rbf(add32(add16(v)));
      const u32 rb = win.y;
      const float r = rbf(__builtin_bit_cast(float, (rb & 0x80000000u) ? (rb & 0x7FFFFFFFu) : ~rb));
      const float sg = rbf(1.f / (1.f + __expf(-r)));
      const float y = rbf(v * sg);
      const float hval = (lane & 1) ? hi16(hw) : lo16(hw), nval = (lane & 1) ? hi16(nw) : lo16(nw);
      const float res = hval + rbf(nval + y);
      const float rn = __shfl(res, l15 | 1);
      if (lane < 16 && !(lane & 1)) *(GAS u32*)(p.out + (size_t)wtok * DM + c0 + lane) = pack2(res, rn);
    }
  }
  STAMP(11);
}

}

extern "C" void kernel_launch(void* const* d_in, const int* in_sizes, int n_in, void* d_out, int out_size, void* d_ws,
                              size_t ws_size, hipStream_t stream) {
  (void)in_sizes; (void)n_in; (void)out_size; (void)ws_size;
  Params p;
  p.x = (const u16*)d_in[0];
  p.wqkv = (const u16*)d_in[1];
  p.wo = (const u16*)d_in[2];
  p.wr = (const u16*)d_in[4];
  p.wg = (const u16*)d_in[5];
  p.wu = (const u16*)d_in[6];
  p.wd = (const u16*)d_in[7];
  p.out = (u16*)d_out;
  p.ws = (unsigned char*)d_ws;
  (void)hipFuncSetAttribute((const void*)fused_block_kernel, hipFuncAttributeMaxDynamicSharedMemorySize, L_BYTES);
  hipLaunchKernelGGL(fused_block_kernel, dim3(NWG), dim3(NT), L_BYTES, stream, p);
}
```

```cpp
#include <hip/hip_runtime.h>
#include <hip/hip_bf16.h>

namespace {

typedef unsigned u32;
typedef unsigned long long u64;
typedef unsigned short u16;
typedef u32 u32x2 __attribute__((ext_vector_type(2)));
typedef u32 u32x4 __attribute__((ext_vector_type(4)));
typedef u32 u32x16 __attribute__((ext_vector_type(16)));
typedef float f32x2 __attribute__((ext_vector_type(2)));
typedef float f32x4 __attribute__((ext_vector_type(4)));
typedef float f32x8 __attribute__((ext_vector_type(8)));
typedef float f32x16 __attribute__((ext_vector_type(16)));
typedef __bf16 bf16x2 __attribute__((ext_vector_type(2)));
typedef __bf16 bf16x8 __attribute__((ext_vector_type(8)));
typedef __bf16 bf16x16 __attribute__((ext_vector_type(16)));
typedef short s16x4 __attribute__((ext_vector_type(4)));
#define LAS __attribute__((address_space(3)))
#define GAS __attribute__((address_space(1)))
#define DEV __device__ __forceinline__

constexpr int DM = 512, FF = 1792;
constexpr int NWG = 256, NT = 512;
constexpr int LH = 1;
constexpr int FSTR = 128;
constexpr int NREP = 32;
constexpr int BSTR = 16;
constexpr int TCODE = 124;

constexpr u32 WS_EPOCH = 0;
constexpr u32 WS_ACT = 4096;
constexpr u32 WS_BEST = 16384;
constexpr u32 WS_FLG = 1u << 17;
constexpr u32 WS_RF = WS_FLG + 32768;
constexpr u32 WS_ST = 1u << 18;
constexpr u32 WS_ROWN = 1u << 19;
constexpr u32 WS_ROWH = WS_ROWN + 64 * 2048;
static_assert(WS_BEST + NREP * 64 * BSTR <= WS_FLG, "ws");
constexpr u32 WS_KV = 1u << 20;
constexpr u32 WS_OB = WS_KV + 32 * 131072;
constexpr u32 WS_BYTES = WS_OB + 64 * 32 * 1024;

constexpr u32 SLOT = 16384;
constexpr u32 L_OWN = 8 * SLOT;
constexpr u32 L_QX = 9 * SLOT;
constexpr u32 L_CMB = 8 * SLOT;
constexpr u32 L_MISC = 9 * SLOT + 8192;
constexpr u32 L_SP = L_MISC;
constexpr u32 L_ST = L_MISC + 2048;
constexpr u32 L_CS = L_MISC + 5632;
constexpr u32 L_WIN = L_MISC + 4864;
constexpr u32 L_BT = L_MISC + 4880;
constexpr u32 L_RDY = L_MISC + 4896;
constexpr u32 L_TOK = L_MISC + 5120;
constexpr u32 L_RED = L_MISC + 5632;
constexpr u32 L_BYTES = 163840;
static_assert(L_RED + 2048 <= L_BYTES && L_CMB + 8 * 3072 <= L_MISC, "lds");
constexpr u32 L_WO = 0;
constexpr u32 L_OI = 0;
constexpr u32 L_PS = 32768;
constexpr u32 L_FP = 65536;
constexpr u32 L_HT = 49152;
constexpr u32 L_ROW = 40960;

constexpr u32 SENT = 0xFFFFFFFFu;
constexpr u32 ONES = 0x3F803F80u;
constexpr int AUX_SC1 = 16;
constexpr u32 SPIN_CAP = 1u << 17;

struct Desc {
  const void* p;
  u32 bytes;
  u32 fmt;
};
enum { D_X, D_WQKV, D_WS, D_WO, D_WG, D_WU, D_WD, D_NULL, D_WR, D_OUT, D_COUNT };
struct Params {
  Desc d[D_COUNT];
};
#define CAS __attribute__((address_space(4)))

DEV u32 pack2(float a, float b) {
  bf16x2 v = {(__bf16)a, (__bf16)b};
  return __builtin_bit_cast(u32, v);
}
DEV float asf(u32 v) { return __builtin_bit_cast(float, v); }
DEV u32 asu(float v) { return __builtin_bit_cast(u32, v); }
DEV float lo16(u32 v) { return asf(v << 16); }
DEV float hi16(u32 v) { return asf(v & 0xFFFF0000u); }
DEV float rbf(float a) { return (float)(__bf16)a; }
DEV u32 clampw(u32 v) { return v < 0xFFFFFFFEu ? v : 0xFFFFFFFEu; }
DEV u32x4 clampw4(u32x4 v) { return u32x4{clampw(v.x), clampw(v.y), clampw(v.z), clampw(v.w)}; }
DEV u32x4 gran(u32 a, u32 b, u64 tag) { return u32x4{a, (u32)tag, b, (u32)(tag >> 32)}; }
DEV int fresh(u32x4 v, u64 tag) { return (v.y == (u32)tag) & (v.w == (u32)(tag >> 32)); }
DEV f32x4 mfma16(u32x4 a, u32x4 b, f32x4 c) {
  return __builtin_amdgcn_mfma_f32_16x16x32_bf16(__builtin_bit_cast(bf16x8, a), __builtin_bit_cast(bf16x8, b), c, 0, 0, 0);
}
DEV f32x16 mfma32(u32x4 a, u32x4 b, f32x16 c) {
  return __builtin_amdgcn_mfma_f32_32x32x16_bf16(__builtin_bit_cast(bf16x8, a), __builtin_bit_cast(bf16x8, b), c, 0, 0, 0);
}
DEV f32x16 smfmac(u32x4 a, f32x8 b, f32x16 c) {
  return __builtin_amdgcn_smfmac_f32_32x32x32_bf16(__builtin_bit_cast(bf16x8, a), __builtin_bit_cast(bf16x16, b), c,
                                                   (int)0xDDDDDDDD, 0, 0);
}
DEV float dot2(u32 a, u32 b, float c) {
  return __builtin_amdgcn_fdot2_f32_bf16(__builtin_bit_cast(bf16x2, a), __builtin_bit_cast(bf16x2, b), c, false);
}
DEV u32x4 lds128(LAS unsigned char* L, u32 off) { return *(LAS u32x4*)(L + off); }
DEV void sts128(LAS unsigned char* L, u32 off, u32x4 v) { *(LAS u32x4*)(L + off) = v; }
DEV u32x2 ldtr(LAS unsigned char* L, u32 off) {
  return __builtin_bit_cast(u32x2, __builtin_amdgcn_ds_read_tr16_b64_v4i16((LAS s16x4*)(L + off)));
}
DEV u32x4 ldtr2(LAS unsigned char* L, u32 off0, u32 off1) {
  u32x2 a = ldtr(L, off0), b = ldtr(L, off1);
  return u32x4{a.x, a.y, b.x, b.y};
}
DEV u32 uni(u32 v) { return __builtin_amdgcn_readfirstlane(v); }
DEV float add16(float v) {
  auto r = __builtin_amdgcn_permlane16_swap(asu(v), asu(v), false, false);
  return asf(r[0]) + asf(r[1]);
}
DEV float add32(float v) {
  auto r = __builtin_amdgcn_permlane32_swap(asu(v), asu(v), false, false);
  return asf(r[0]) + asf(r[1]);
}
DEV float max32(float v) {
  auto r = __builtin_amdgcn_permlane32_swap(asu(v), asu(v), false, false);
  return fmaxf(asf(r[0]), asf(r[1]));
}
DEV float max16x(f32x16 s) {
  float a = fmaxf(fmaxf(s[0], s[1]), s[2]), b = fmaxf(fmaxf(s[3], s[4]), s[5]), c = fmaxf(fmaxf(s[6], s[7]), s[8]);
  float d = fmaxf(fmaxf(s[9], s[10]), s[11]), e = fmaxf(fmaxf(s[12], s[13]), s[14]);
  return fmaxf(fmaxf(fmaxf(a, b), c), fmaxf(fmaxf(d, e), s[15]));
}
DEV f32x16 bcast16(float v) { return f32x16{v, v, v, v, v, v, v, v, v, v, v, v, v, v, v, v}; }
template <int CTRL>
DEV u32 dppmax(u32 v) { return max(v, (u32)__builtin_amdgcn_update_dpp(0, (int)v, CTRL, 0xF, 0xF, true)); }
DEV u32 half_max(u32 v) {
  v = dppmax<0x128>(dppmax<0x124>(dppmax<0x122>(dppmax<0x121>(v))));
  auto r = __builtin_amdgcn_permlane16_swap(v, v, false, false);
  return max((u32)r[0], (u32)r[1]);
}
DEV u32 wave_max(u32 v) {
  v = half_max(v);
  auto r = __builtin_amdgcn_permlane32_swap(v, v, false, false);
  return max((u32)r[0], (u32)r[1]);
}
DEV u32x4 join16(u32x2 a, u32x2 b) {
  auto r0 = __builtin_amdgcn_permlane16_swap(a.x, b.x, false, false);
  auto r1 = __builtin_amdgcn_permlane16_swap(a.y, b.y, false, false);
  return u32x4{(u32)r0[0], (u32)r1[0], (u32)r0[1], (u32)r1[1]};
}
DEV f32x2 ln_scalars(float s1, float s2) {
  const float mean = s1 * (1.f / DM);
  const float var = fmaxf(s2 * (1.f / DM) - mean * mean, 0.f);
  return f32x2{rbf(mean), __builtin_amdgcn_rcpf(rbf(__builtin_amdgcn_sqrtf(rbf(rbf(var) + 1e-5f))))};
}
DEV u32 ln_pair(u32 h, float mean, float rstd) {
  const u32 c = pack2(lo16(h) - mean, hi16(h) - mean);
  return pack2(lo16(c) * rstd, hi16(c) * rstd);
}
template <class T, class LD, class OK>
DEV void poll(T& a, LD load, OK ok) {
#pragma unroll 1
  for (u32 spin = 0; spin < SPIN_CAP; ++spin) {
    asm volatile("" ::: "memory");
    load(a);
    __builtin_amdgcn_sched_barrier(0);
    if (ok(a)) break;
  }
}
struct V4 { u32x4 v[4]; };
struct V3 { u32x4 v[3]; u32 c; };
DEV u32 max4(u32x4 v) { return max(max(v.x, v.y), max(v.z, v.w)); }
#define VMWAIT(n) asm volatile("s_waitcnt vmcnt(" #n ")" ::: "memory")
DEV void lds_barrier() {
  asm volatile("s_waitcnt lgkmcnt(0)" ::: "memory");
  __builtin_amdgcn_s_barrier();
  asm volatile("" ::: "memory");
}


__global__ void __launch_bounds__(NT, 2) fused_block_kernel(Params) {
  const CAS u32x4* const dt = (const CAS u32x4*)__builtin_amdgcn_kernarg_segment_ptr();
  u32x4 rx, rw, rws;
  asm volatile("s_load_dwordx8 s[72:79], %3, 0x0\n\ts_load_dwordx4 s[80:83], %3, 0x20" : "={s[72:75]}"(rx), "={s[76:79]}"(rw), "={s[80:83]}"(rws) : "s"(dt));
  __builtin_amdgcn_sched_barrier(0);
  extern __shared__ __attribute__((aligned(16))) unsigned char smem[];
  LAS unsigned char* L = (LAS unsigned char*)smem;
  const u32 lb = (u32)(size_t)L;
  int lane = threadIdx.x & 63;
  int w = __builtin_amdgcn_readfirstlane(threadIdx.x >> 6);
  const int bid = blockIdx.x;
  const int xg = bid & 7, xi = bid >> 3;
  const int li = xi >> LH, hg = ((xg >> LH) << LH) + (xi & ((1 << LH) - 1)), tg = (32 >> LH) * (xg & ((1 << LH) - 1)) + li;
  int l15 = lane & 15, g = lane >> 4, l31 = lane & 31, hh = lane >> 5;
#define RELANE()                                                                        \
  do {                                                                                  \
    lane = __builtin_amdgcn_mbcnt_hi(~0u, __builtin_amdgcn_mbcnt_lo(~0u, 0u));          \
    asm volatile("" : "+v"(lane), "+s"(w));                                             \
    w = __builtin_amdgcn_readfirstlane(w);                                              \
    l15 = lane & 15; g = lane >> 4; l31 = lane & 31; hh = lane >> 5;                    \
  } while (0)
  const int panel = (64 >> LH) * (xg & ((1 << LH) - 1)) + 8 * (xg >> LH) + (xi >> 2), cq = xi & 3;
  const int j = w & 3, th = w >> 2;
  u64 epoch;

  const bool role_gu = xi < 7, role_dn = xi >= 7 && xi < 11;
  const int ug = 7 * xg + xi, dc = 64 * xg + 16 * (xi - 7);
  const u32x4 ones4 = {ONES, ONES, ONES, ONES};
  const u32 lw = (8 * w + (lane >> 3)) * 3072 + hg * 128 + ((((lane >> 1) & 3) ^ ((g & 1) | ((w & 1) << 1))) * 32) + (lane & 1) * 16;
  const u32 lx = (64 * tg + 8 * w + (lane >> 3)) * 1024 + (((lane & 7) ^ (g | ((w & 1) << 2))) * 16);
  const u32 ml = lb + w * 1024;
  u32 ck = (li + 4 * hg) & 7, st, su, d0;
  asm volatile(
      "s_waitcnt lgkmcnt(0)\n"
      "s_nop 3\n"
      "s_lshl_b32 %[st], %[ck], 7\n"
      "s_add_u32 m0, %[ml], 0\n"
      "s_mul_i32 %[su], %[ck], 0x30000\n"
      "buffer_load_dwordx4 %[lx], %[rx], %[st] offen lds\n"
      "s_add_u32 m0, %[ml], 7168\n"
      "s_add_u32 %[ck], %[ck], 1\n"
      "buffer_load_dwordx4 %[lw], %[rw], %[su] offen offset:1024 lds\n"
      "s_add_u32 m0, %[ml], 14336\n"
      "s_and_b32 %[ck], %[ck], 7\n"
      "buffer_load_dwordx4 %[lw], %[rw], %[su] offen offset:2048 lds\n"
      "s_getpc_b64 vcc\n"
      "s_load_dword %[d0], vcc, %[tc]\n"
      "s_load_dword %[d0], vcc, %[tc] offset:11904\n"
      "s_lshl_b32 %[st], %[ck], 7\n"
      "s_add_u32 m0, %[ml], 24576\n"
      "s_mul_i32 %[su], %[ck], 0x30000\n"
      "buffer_load_dwordx4 %[lx], %[rx], %[st] offen lds\n"
      "s_add_u32 m0, %[ml], 31744\n"
      "s_add_u32 %[ck], %[ck], 1\n"
      "buffer_load_dwordx4 %[lw], %[rw], %[su] offen offset:1024 lds\n"
      "s_add_u32 m0, %[ml], 38912\n"
      "s_and_b32 %[ck], %[ck], 7\n"
      "buffer_load_dwordx4 %[lw], %[rw], %[su] offen offset:2048 lds\n"
      "s_lshl_b32 %[st], %[ck], 7\n"
      "s_add_u32 m0, %[ml], 49152\n"
      "s_mul_i32 %[su], %[ck], 0x30000\n"
      "buffer_load_dwordx4 %[lx], %[rx], %[st] offen lds\n"
      "s_add_u32 m0, %[ml], 56320\n"
      "s_add_u32 %[ck], %[ck], 1\n"
      "buffer_load_dwordx4 %[lw], %[rw], %[su] offen offset:1024 lds\n"
      "s_add_u32 m0, %[ml], 63488\n"
      "s_and_b32 %[ck], %[ck], 7\n"
      "buffer_load_dwordx4 %[lw], %[rw], %[su] offen offset:2048 lds\n"
      : [st] "=&s"(st), [su] "=&s"(su), [ck] "+s"(ck), [d0] "=&{s68}"(d0), [rx] "+{s[72:75]}"(rx), [rw] "+{s[76:79]}"(rw), "+{s[80:83]}"(rws)
      : [lx] "v"(lx), [lw] "v"(lw), [ml] "s"(ml), [tc] "s"(min(xi * 8 + w, 185) * 64)
      : "memory", "scc", "vcc");
  unsigned char* const pws = (unsigned char*)(((u64)uni(rws.y) << 32) | uni(rws.x));
  const __amdgpu_buffer_rsrc_t rs = __builtin_amdgcn_make_buffer_rsrc(pws, 0, WS_BYTES, 0x00020000);
  u32 so, wm, fs, fo2, to0, to1, to2, sob, srf;
  u32x4 va0, va1, va2;
  {
    const u32 kvb = WS_KV + (4 * hg + j) * 131072 + th * 2048;
    const u32 kof = kvb + lane * 16, vof = kvb + hh * 256 + l15 * 16;
    so = ((tg + 1) & 31) * 4096;
    const u32 ov0 = (u32)(64 * w * 1024 + cq * 256 + g * 1024 + (((l15 >> 1) ^ g) * 32) + (lane & 1) * 16), ov1 = (ov0 ^ 128u) + 4096u;
    wm = lb + L_WO + 64 * w * 256;
    const u32 fv = role_gu ? (64 * w + (lane >> 2)) * (FF * 2) + ug * 64 + (lane & 3) * 16
                           : (224 * w + (lane >> 1)) * 1024 + dc * 2 + (lane & 1) * 16;
    fs = role_gu ? 16 * FF * 2 : 32768;
    fo2 = role_gu ? 0 : 4 * 32768;
    const u32 wrv = (128 * cq + 16 * w + 8 * hh) * 2, xrv = (32 * panel + l15) * 1024 + (128 * cq + 16 * w + 4 * g) * 2;
    to0 = (16 * xi + 8 * th) * 1024 + (xg >> 1) * 256 + j * 64;
    to1 = 16 * xi * (FF * 2) + 448 * xg + 64 * w;
    to2 = 14 * (4 * xi + (w >> 1)) * 1024 + 128 * xg + 64 * (w & 1);
    const u32 otile = (2 * tg + th) * 32 + 4 * hg + j;
    sob = WS_OB + otile * 1024;
    srf = WS_RF + ((2 * tg + th) * 8 + hg) * 128 + j * 8;
    const u32 rfv = WS_RF + (panel * 8 + w) * 128 + (lane & 3) * 8;
    const u32 onev = (l31 | 4) == 20 ? ONES : 0u;
    const u32 flv = WS_FLG + (hg * 32 + ((tg + l31) & 31)) * FSTR;
    fs = uni(fs); fo2 = uni(fo2); sob = uni(sob); srf = uni(srf); so = uni(so); wm = uni(wm); to0 = uni(to0); to1 = uni(to1); to2 = uni(to2);
    asm volatile("" : "+s"(fs), "+s"(fo2), "+s"(sob), "+s"(srf), "+s"(so), "+s"(wm), "+s"(to0), "+s"(to1), "+s"(to2));
    va0 = u32x4{kof, vof, ov0, ov1};
    va1 = u32x4{fv, xrv, wrv, rfv};
    va2 = u32x4{onev, flv, (u32)lane * 16, lb + L_RDY};
    asm volatile("" : "+{v[16:19]}"(va0), "+{v[20:23]}"(va1), "+{v[24:27]}"(va2));
  }
  if (threadIdx.x == 0) *(LAS u32x2*)(L + L_RDY) = u32x2{0u, 0u};
  u32x16 rw4;
  u32x4 rwr, rfl, rf0, rf1;
  u64 pout;
  u32 e_sp = L_SP + (32 * th + (l31 ^ (16 * (j >> 1)))) * 8, e_cs = L_CS + j * 192 + g * 16, e_cv = L_CS + j * 192 + 128 + l15 * 4;
  u32 e_s4 = L_ST + w * 256 + g * 16;
  u32 e_in = (g & 1) ? 1024 + (j >> 1) * 512 + hh * 256 + l15 * 16 : hh * 512 + (16 * (j >> 1) + l15) * 16;
  u32 e_w = L_SP + ((j & 1) * 64 + 32 * th + 16 * (j >> 1) + l15) * 8;
  asm volatile("" : "+v"(e_sp), "+v"(e_cs), "+v"(e_cv), "+v"(e_s4), "+v"(e_in), "+v"(e_w));
  f32x4 ak0, ak1, av0, av1, aq0, aq1, csk, csv, csq;
  f32x2 st2;
  u32x16 x0, x1, x2, x3, x4, x5, x6, x7;
  u32x4 wa, one_r;
  const int tx = j >> 1;
  {
    u32x4 xa;
    const u32 wra = lb + (8 * g + ((lane >> 2) & 3)) * 128 + ((j ^ (((lane >> 3) & 1) | ((g & 1) << 1))) * 32) + (lane & 3) * 8;
#pragma unroll
    for (int i = 0; i < 2; ++i) {
      const int k2 = (i + j + th) & 1;
      const u32 a = lb + (32 * th + l15) * 128 + (((4 * k2 + g) ^ (l15 >> 1)) * 16);
      xa[2 * i] = a + tx * 2048;
      xa[2 * i + 1] = a + (tx ^ 1) * 2048;
      wa[i] = wra + k2 * 4096;
      wa[2 + i] = wa[i] + 49152;
    }
    const u32x4 xb = xa + 49152;
    asm volatile(
      "s_nop 4\n"
      "buffer_load_dwordx2 v[88:89], off, %[rws], 0 sc1\n"
      "s_load_dwordx16 s[52:67], %[dt], 0x30\n"
      "s_load_dwordx4 %[rwr], %[dt], 0x80\n"
      "s_load_dwordx2 %[po], %[dt], 0x90\n"
      "s_load_dwordx4 %[rfl], %[dt], %[ifl]\n"
      "s_load_dwordx4 %[rf0], %[dt], %[if0]\n"
      "s_load_dwordx4 %[rf1], %[dt], %[if1]\n"
      "v_mov_b32 v92, 0x3f803f80\n"
      "v_mov_b32 v93, 0x3f803f80\n"
      "v_mov_b32 v94, 0x3f803f80\n"
      "v_mov_b32 v95, 0x3f803f80\n"
      "v_mov_b32 v60, 0\n"
      "v_mov_b32 v61, 0\n"
      "s_waitcnt vmcnt(7)\n"
      "s_barrier\n"
      "ds_read_b64_tr_b16 v[96:97], v40 offset:8192\n"
      "ds_read_b64_tr_b16 v[98:99], v40 offset:8704\n"
      "ds_read_b128 v[128:131], v32 offset:0\n"
      "ds_read_b128 v[132:135], v33 offset:0\n"
      "ds_read_b64_tr_b16 v[104:105], v40 offset:16384\n"
      "ds_read_b64_tr_b16 v[106:107], v40 offset:16896\n"
      "ds_read_b64_tr_b16 v[100:101], v41 offset:8192\n"
      "ds_read_b64_tr_b16 v[102:103], v41 offset:8704\n"
      "ds_read_b128 v[136:139], v34 offset:0\n"
      "ds_read_b128 v[140:143], v35 offset:0\n"
      "ds_read_b64_tr_b16 v[108:109], v41 offset:16384\n"
      "ds_read_b64_tr_b16 v[110:111], v41 offset:16896\n"
      "s_lshl_b32 %[st], %[ck], 7\n"
      "s_add_u32 m0, %[ml], 73728\n"
      "s_mul_i32 %[su], %[ck], 0x30000\n"
      "buffer_load_dwordx4 %[lx], %[rx], %[st] offen lds\n"
      "s_add_u32 m0, %[ml], 80896\n"
      "s_add_u32 %[ck], %[ck], 1\n"
      "buffer_load_dwordx4 %[lw], %[rw], %[su] offen offset:1024 lds\n"
      "s_add_u32 m0, %[ml], 88064\n"
      "s_and_b32 %[ck], %[ck], 7\n"
      "buffer_load_dwordx4 %[lw], %[rw], %[su] offen offset:2048 lds\n"
      "s_waitcnt vmcnt(7) lgkmcnt(0)\n"
      "s_barrier\n"
      "v_mfma_f32_16x16x32_bf16 v[64:67], v[96:99], v[128:131], 0\n"
      "ds_read_b64_tr_b16 v[112:113], v40 offset:32768\n"
      "ds_read_b64_tr_b16 v[114:115], v40 offset:33280\n"
      "s_lshl_b32 %[st], %[ck], 7\n"
      "s_add_u32 m0, %[ml], 0\n"
      "s_mul_i32 %[su], %[ck], 0x30000\n"
      "buffer_load_dwordx4 %[lx], %[rx], %[st] offen lds\n"
      "v_mfma_f32_16x16x32_bf16 v[68:71], v[96:99], v[132:135], 0\n"
      "ds_read_b128 v[144:147], v32 offset:24576\n"
      "ds_read_b128 v[148:151], v33 offset:24576\n"
      "v_mfma_f32_16x16x32_bf16 v[48:51], v[96:99], v[92:95], 0\n"
      "ds_read_b64_tr_b16 v[120:121], v40 offset:40960\n"
      "ds_read_b64_tr_b16 v[122:123], v40 offset:41472\n"
      "v_lshlrev_b32 v62, 16, v128\n"
      "v_and_b32 v63, 0xffff0000, v128\n"
      "v_add_f32 v60, v60, v62\n"
      "v_mfma_f32_16x16x32_bf16 v[72:75], v[128:131], v[104:107], 0\n"
      "ds_read_b64_tr_b16 v[116:117], v41 offset:32768\n"
      "ds_read_b64_tr_b16 v[118:119], v41 offset:33280\n"
      "v_fmac_f32 v61, v62, v62\n"
      "v_add_f32 v60, v60, v63\n"
      "v_fmac_f32 v61, v63, v63\n"
      "s_add_u32 m0, %[ml], 7168\n"
      "s_add_u32 %[ck], %[ck], 1\n"
      "buffer_load_dwordx4 %[lw], %[rw], %[su] offen offset:1024 lds\n"
      "v_mfma_f32_16x16x32_bf16 v[76:79], v[132:135], v[104:107], 0\n"
      "ds_read_b128 v[152:155], v34 offset:24576\n"
      "ds_read_b128 v[156:159], v35 offset:24576\n"
      "v_lshlrev_b32 v62, 16, v129\n"
      "v_and_b32 v63, 0xffff0000, v129\n"
      "v_add_f32 v60, v60, v62\n"
      "v_mfma_f32_16x16x32_bf16 v[52:55], v[92:95], v[104:107], 0\n"
      "ds_read_b64_tr_b16 v[124:125], v41 offset:40960\n"
      "ds_read_b64_tr_b16 v[126:127], v41 offset:41472\n"
      "v_fmac_f32 v61, v62, v62\n"
      "v_add_f32 v60, v60, v63\n"
      "v_fmac_f32 v61, v63, v63\n"
      "v_mfma_f32_16x16x32_bf16 v[64:67], v[100:103], v[136:139], v[64:67]\n"
      "v_lshlrev_b32 v62, 16, v130\n"
      "v_and_b32 v63, 0xffff0000, v130\n"
      "v_add_f32 v60, v60, v62\n"
      "s_add_u32 m0, %[ml], 14336\n"
      "s_and_b32 %[ck], %[ck], 7\n"
      "buffer_load_dwordx4 %[lw], %[rw], %[su] offen offset:2048 lds\n"
      "v_mfma_f32_16x16x32_bf16 v[68:71], v[100:103], v[140:143], v[68:71]\n"
      "v_fmac_f32 v61, v62, v62\n"
      "v_add_f32 v60, v60, v63\n"
      "v_fmac_f32 v61, v63, v63\n"
      "v_mfma_f32_16x16x32_bf16 v[72:75], v[136:139], v[108:111], v[72:75]\n"
      "v_lshlrev_b32 v62, 16, v131\n"
      "v_and_b32 v63, 0xffff0000, v131\n"
      "v_add_f32 v60, v60, v62\n"
      "v_mfma_f32_16x16x32_bf16 v[76:79], v[140:143], v[108:111], v[76:79]\n"
      "v_fmac_f32 v61, v62, v62\n"
      "v_add_f32 v60, v60, v63\n"
      "v_fmac_f32 v61, v63, v63\n"
      "s_waitcnt vmcnt(7) lgkmcnt(0)\n"
      "s_barrier\n"
      "v_mfma_f32_16x16x32_bf16 v[64:67], v[112:115], v[144:147], v[64:67]\n"
      "ds_read_b64_tr_b16 v[96:97], v42 offset:8192\n"
      "ds_read_b64_tr_b16 v[98:99], v42 offset:8704\n"
      "s_lshl_b32 %[st], %[ck], 7\n"
      "s_add_u32 m0, %[ml], 24576\n"
      "s_mul_i32 %[su], %[ck], 0x30000\n"
      "buffer_load_dwordx4 %[lx], %[rx], %[st] offen lds\n"
      "v_mfma_f32_16x16x32_bf16 v[68:71], v[112:115], v[148:151], v[68:71]\n"
      "ds_read_b128 v[160:163], v36 offset:0\n"
      "ds_read_b128 v[164:167], v37 offset:0\n"
      "v_mfma_f32_16x16x32_bf16 v[48:51], v[112:115], v[92:95], v[48:51]\n"
      "ds_read_b64_tr_b16 v[104:105], v42 offset:16384\n"
      "ds_read_b64_tr_b16 v[106:107], v42 offset:16896\n"
      "v_lshlrev_b32 v62, 16, v144\n"
      "v_and_b32 v63, 0xffff0000, v144\n"
      "v_add_f32 v60, v60, v62\n"
      "v_mfma_f32_16x16x32_bf16 v[72:75], v[144:147], v[120:123], v[72:75]\n"
      "ds_read_b64_tr_b16 v[100:101], v43 offset:8192\n"
      "ds_read_b64_tr_b16 v[102:103], v43 offset:8704\n"
      "v_fmac_f32 v61, v62, v62\n"
      "v_add_f32 v60, v60, v63\n"
      "v_fmac_f32 v61, v63, v63\n"
      "s_add_u32 m0, %[ml], 31744\n"
      "s_add_u32 %[ck], %[ck], 1\n"
      "buffer_load_dwordx4 %[lw], %[rw], %[su] offen offset:1024 lds\n"
      "v_mfma_f32_16x16x32_bf16 v[76:79], v[148:151], v[120:123], v[76:79]\n"
      "ds_read_b128 v[168:171], v38 offset:0\n"
      "ds_read_b128 v[172:175], v39 offset:0\n"
      "v_lshlrev_b32 v62, 16, v145\n"
      "v_and_b32 v63, 0xffff0000, v145\n"
      "v_add_f32 v60, v60, v62\n"
      "v_mfma_f32_16x16x32_bf16 v[52:55], v[92:95], v[120:123], v[52:55]\n"
      "ds_read_b64_tr_b16 v[108:109], v43 offset:16384\n"
      "ds_read_b64_tr_b16 v[110:111], v43 offset:16896\n"
      "v_fmac_f32 v61, v62, v62\n"
      "v_add_f32 v60, v60, v63\n"
      "v_fmac_f32 v61, v63, v63\n"
      "v_mfma_f32_16x16x32_bf16 v[64:67], v[116:119], v[152:155], v[64:67]\n"
      "v_lshlrev_b32 v62, 16, v146\n"
      "v_and_b32 v63, 0xffff0000, v146\n"
      "v_add_f32 v60, v60, v62\n"
      "s_add_u32 m0, %[ml], 38912\n"
      "s_and_b32 %[ck], %[ck], 7\n"
      "buffer_load_dwordx4 %[lw], %[rw], %[su] offen offset:2048 lds\n"
      "v_mfma_f32_16x16x32_bf16 v[68:71], v[116:119], v[156:159], v[68:71]\n"
      "v_fmac_f32 v61, v62, v62\n"
      "v_add_f32 v60, v60, v63\n"
      "v_fmac_f32 v61, v63, v63\n"
      "v_mfma_f32_16x16x32_bf16 v[72:75], v[152:155], v[124:127], v[72:75]\n"
      "v_lshlrev_b32 v62, 16, v147\n"
      "v_and_b32 v63, 0xffff0000, v147\n"
      "v_add_f32 v60, v60, v62\n"
      "v_mfma_f32_16x16x32_bf16 v[76:79], v[156:159], v[124:127], v[76:79]\n"
      "v_fmac_f32 v61, v62, v62\n"
      "v_add_f32 v60, v60, v63\n"
      "v_fmac_f32 v61, v63, v63\n"
      "s_waitcnt vmcnt(6) lgkmcnt(0)\n"
      "s_barrier\n"
      "v_mfma_f32_16x16x32_bf16 v[64:67], v[96:99], v[160:163], v[64:67]\n"
      "ds_read_b64_tr_b16 v[112:113], v42 offset:32768\n"
      "ds_read_b64_tr_b16 v[114:115], v42 offset:33280\n"
      "s_lshl_b32 %[st], %[ck], 7\n"
      "s_add_u32 m0, %[ml], 49152\n"
      "s_mul_i32 %[su], %[ck], 0x30000\n"
      "buffer_load_dwordx4 %[lx], %[rx], %[st] offen lds\n"
      "v_mfma_f32_16x16x32_bf16 v[68:71], v[96:99], v[164:167], v[68:71]\n"
      "ds_read_b128 v[176:179], v36 offset:24576\n"
      "ds_read_b128 v[180:183], v37 offset:24576\n"
      "v_mfma_f32_16x16x32_bf16 v[48:51], v[96:99], v[92:95], v[48:51]\n"
      "ds_read_b64_tr_b16 v[120:121], v42 offset:40960\n"
      "ds_read_b64_tr_b16 v[122:123], v42 offset:41472\n"
      "v_lshlrev_b32 v62, 16, v160\n"
      "v_and_b32 v63, 0xffff0000, v160\n"
      "v_add_f32 v60, v60, v62\n"
      "v_mfma_f32_16x16x32_bf16 v[72:75], v[160:163], v[104:107], v[72:75]\n"
      "ds_read_b64_tr_b16 v[116:117], v43 offset:32768\n"
      "ds_read_b64_tr_b16 v[118:119], v43 offset:33280\n"
      "v_fmac_f32 v61, v62, v62\n"
      "v_add_f32 v60, v60, v63\n"
      "v_fmac_f32 v61, v63, v63\n"
      "s_add_u32 m0, %[ml], 56320\n"
      "s_add_u32 %[ck], %[ck], 1\n"
      "buffer_load_dwordx4 %[lw], %[rw], %[su] offen offset:1024 lds\n"
      "v_mfma_f32_16x16x32_bf16 v[76:79], v[164:167], v[104:107], v[76:79]\n"
      "ds_read_b128 v[184:187], v38 offset:24576\n"
      "ds_read_b128 v[188:191], v39 offset:24576\n"
      "v_lshlrev_b32 v62, 16, v161\n"
      "v_and_b32 v63, 0xffff0000, v161\n"
      "v_add_f32 v60, v60, v62\n"
      "v_mfma_f32_16x16x32_bf16 v[52:55], v[92:95], v[104:107], v[52:55]\n"
      "ds_read_b64_tr_b16 v[124:125], v43 offset:40960\n"
      "ds_read_b64_tr_b16 v[126:127], v43 offset:41472\n"
      "v_fmac_f32 v61, v62, v62\n"
      "v_add_f32 v60, v60, v63\n"
      "v_fmac_f32 v61, v63, v63\n"
      "v_mfma_f32_16x16x32_bf16 v[64:67], v[100:103], v[168:171], v[64:67]\n"
      "v_lshlrev_b32 v62, 16, v162\n"
      "v_and_b32 v63, 0xffff0000, v162\n"
      "v_add_f32 v60, v60, v62\n"
      "s_add_u32 m0, %[ml], 63488\n"
      "s_and_b32 %[ck], %[ck], 7\n"
      "buffer_load_dwordx4 %[lw], %[rw], %[su] offen offset:2048 lds\n"
      "v_mfma_f32_16x16x32_bf16 v[68:71], v[100:103], v[172:175], v[68:71]\n"
      "v_fmac_f32 v61, v62, v62\n"
      "v_add_f32 v60, v60, v63\n"
      "v_fmac_f32 v61, v63, v63\n"
      "v_mfma_f32_16x16x32_bf16 v[72:75], v[168:171], v[108:111], v[72:75]\n"
      "v_lshlrev_b32 v62, 16, v163\n"
      "v_and_b32 v63, 0xffff0000, v163\n"
      "v_add_f32 v60, v60, v62\n"
      "v_mfma_f32_16x16x32_bf16 v[76:79], v[172:175], v[108:111], v[76:79]\n"
      "v_fmac_f32 v61, v62, v62\n"
      "v_add_f32 v60, v60, v63\n"
      "v_fmac_f32 v61, v63, v63\n"
      "s_waitcnt vmcnt(6) lgkmcnt(0)\n"
      "s_barrier\n"
      "v_mfma_f32_16x16x32_bf16 v[64:67], v[112:115], v[176:179], v[64:67]\n"
      "ds_read_b64_tr_b16 v[96:97], v40 offset:8192\n"
      "ds_read_b64_tr_b16 v[98:99], v40 offset:8704\n"
      "s_lshl_b32 %[st], %[ck], 7\n"
      "s_add_u32 m0, %[ml], 73728\n"
      "s_mul_i32 %[su], %[ck], 0x30000\n"
      "buffer_load_dwordx4 %[lx], %[rx], %[st] offen lds\n"
      "v_mfma_f32_16x16x32_bf16 v[68:71], v[112:115], v[180:183], v[68:71]\n"
      "ds_read_b128 v[192:195], v32 offset:0\n"
      "ds_read_b128 v[196:199], v33 offset:0\n"
      "v_mfma_f32_16x16x32_bf16 v[48:51], v[112:115], v[92:95], v[48:51]\n"
      "ds_read_b64_tr_b16 v[104:105], v40 offset:16384\n"
      "ds_read_b64_tr_b16 v[106:107], v40 offset:16896\n"
      "v_lshlrev_b32 v62, 16, v176\n"
      "v_and_b32 v63, 0xffff0000, v176\n"
      "v_add_f32 v60, v60, v62\n"
      "v_mfma_f32_16x16x32_bf16 v[72:75], v[176:179], v[120:123], v[72:75]\n"
      "ds_read_b64_tr_b16 v[100:101], v41 offset:8192\n"
      "ds_read_b64_tr_b16 v[102:103], v41 offset:8704\n"
      "v_fmac_f32 v61, v62, v62\n"
      "v_add_f32 v60, v60, v63\n"
      "v_fmac_f32 v61, v63, v63\n"
      "s_add_u32 m0, %[ml], 80896\n"
      "s_add_u32 %[ck], %[ck], 1\n"
      "buffer_load_dwordx4 %[lw], %[rw], %[su] offen offset:1024 lds\n"
      "v_mfma_f32_16x16x32_bf16 v[76:79], v[180:183], v[120:123], v[76:79]\n"
      "ds_read_b128 v[200:203], v34 offset:0\n"
      "ds_read_b128 v[204:207], v35 offset:0\n"
      "v_lshlrev_b32 v62, 16, v177\n"
      "v_and_b32 v63, 0xffff0000, v177\n"
      "v_add_f32 v60, v60, v62\n"
      "v_mfma_f32_16x16x32_bf16 v[52:55], v[92:95], v[120:123], v[52:55]\n"
      "ds_read_b64_tr_b16 v[108:109], v41 offset:16384\n"
      "ds_read_b64_tr_b16 v[110:111], v41 offset:16896\n"
      "v_fmac_f32 v61, v62, v62\n"
      "v_add_f32 v60, v60, v63\n"
      "v_fmac_f32 v61, v63, v63\n"
      "v_mfma_f32_16x16x32_bf16 v[64:67], v[116:119], v[184:187], v[64:67]\n"
      "v_lshlrev_b32 v62, 16, v178\n"
      "v_and_b32 v63, 0xffff0000, v178\n"
      "v_add_f32 v60, v60, v62\n"
      "s_add_u32 m0, %[ml], 88064\n"
      "s_and_b32 %[ck], %[ck], 7\n"
      "buffer_load_dwordx4 %[lw], %[rw], %[su] offen offset:2048 lds\n"
      "v_mfma_f32_16x16x32_bf16 v[68:71], v[116:119], v[188:191], v[68:71]\n"
      "v_fmac_f32 v61, v62, v62\n"
      "v_add_f32 v60, v60, v63\n"
      "v_fmac_f32 v61, v63, v63\n"
      "v_mfma_f32_16x16x32_bf16 v[72:75], v[184:187], v[124:127], v[72:75]\n"
      "v_lshlrev_b32 v62, 16, v179\n"
      "v_and_b32 v63, 0xffff0000, v179\n"
      "v_add_f32 v60, v60, v62\n"
      "v_mfma_f32_16x16x32_bf16 v[76:79], v[188:191], v[124:127], v[76:79]\n"
      "v_fmac_f32 v61, v62, v62\n"
      "v_add_f32 v60, v60, v63\n"
      "v_fmac_f32 v61, v63, v63\n"
      "s_waitcnt vmcnt(6) lgkmcnt(0)\n"
      "s_barrier\n"
      "v_mfma_f32_16x16x32_bf16 v[64:67], v[96:99], v[192:195], v[64:67]\n"
      "ds_read_b64_tr_b16 v[112:113], v40 offset:32768\n"
      "ds_read_b64_tr_b16 v[114:115], v40 offset:33280\n"
      "s_mul_i32 %[su], %[ck], 0x30000\n"
      "s_add_u32 m0, %[ml], 0\n"
      "s_add_u32 %[ck], %[ck], 1\n"
      "buffer_load_dwordx4 %[lw], %[rw], %[su] offen lds\n"
      "v_mfma_f32_16x16x32_bf16 v[68:71], v[96:99], v[196:199], v[68:71]\n"
      "ds_read_b128 v[208:211], v32 offset:24576\n"
      "ds_read_b128 v[212:215], v33 offset:24576\n"
      "v_mfma_f32_16x16x32_bf16 v[48:51], v[96:99], v[92:95], v[48:51]\n"
      "ds_read_b64_tr_b16 v[120:121], v40 offset:40960\n"
      "ds_read_b64_tr_b16 v[122:123], v40 offset:41472\n"
      "v_lshlrev_b32 v62, 16, v192\n"
      "v_and_b32 v63, 0xffff0000, v192\n"
      "v_add_f32 v60, v60, v62\n"
      "v_mfma_f32_16x16x32_bf16 v[72:75], v[192:195], v[104:107], v[72:75]\n"
      "ds_read_b64_tr_b16 v[116:117], v41 offset:32768\n"
      "ds_read_b64_tr_b16 v[118:119], v41 offset:33280\n"
      "v_fmac_f32 v61, v62, v62\n"
      "v_add_f32 v60, v60, v63\n"
      "v_fmac_f32 v61, v63, v63\n"
      "s_and_b32 %[ck], %[ck], 7\n"
      "s_mul_i32 %[su], %[ck], 0x30000\n"
      "s_add_u32 m0, %[ml], 8192\n"
      "s_add_u32 %[ck], %[ck], 1\n"
      "buffer_load_dwordx4 %[lw], %[rw], %[su] offen lds\n"
      "v_mfma_f32_16x16x32_bf16 v[76:79], v[196:199], v[104:107], v[76:79]\n"
      "ds_read_b128 v[216:219], v34 offset:24576\n"
      "ds_read_b128 v[220:223], v35 offset:24576\n"
      "v_lshlrev_b32 v62, 16, v193\n"
      "v_and_b32 v63, 0xffff0000, v193\n"
      "v_add_f32 v60, v60, v62\n"
      "v_mfma_f32_16x16x32_bf16 v[52:55], v[92:95], v[104:107], v[52:55]\n"
      "ds_read_b64_tr_b16 v[124:125], v41 offset:40960\n"
      "ds_read_b64_tr_b16 v[126:127], v41 offset:41472\n"
      "v_fmac_f32 v61, v62, v62\n"
      "v_add_f32 v60, v60, v63\n"
      "v_fmac_f32 v61, v63, v63\n"
      "v_mfma_f32_16x16x32_bf16 v[64:67], v[100:103], v[200:203], v[64:67]\n"
      "v_lshlrev_b32 v62, 16, v194\n"
      "v_and_b32 v63, 0xffff0000, v194\n"
      "v_add_f32 v60, v60, v62\n"
      "s_and_b32 %[ck], %[ck], 7\n"
      "s_mul_i32 %[su], %[ck], 0x30000\n"
      "s_add_u32 m0, %[ml], 16384\n"
      "s_add_u32 %[ck], %[ck], 1\n"
      "buffer_load_dwordx4 %[lw], %[rw], %[su] offen lds\n"
      "s_and_b32 %[ck], %[ck], 7\n"
      "v_mfma_f32_16x16x32_bf16 v[68:71], v[100:103], v[204:207], v[68:71]\n"
      "v_fmac_f32 v61, v62, v62\n"
      "v_add_f32 v60, v60, v63\n"
      "v_fmac_f32 v61, v63, v63\n"
      "v_mfma_f32_16x16x32_bf16 v[72:75], v[200:203], v[108:111], v[72:75]\n"
      "v_lshlrev_b32 v62, 16, v195\n"
      "v_and_b32 v63, 0xffff0000, v195\n"
      "v_add_f32 v60, v60, v62\n"
      "v_mfma_f32_16x16x32_bf16 v[76:79], v[204:207], v[108:111], v[76:79]\n"
      "v_fmac_f32 v61, v62, v62\n"
      "v_add_f32 v60, v60, v63\n"
      "v_fmac_f32 v61, v63, v63\n"
      "s_waitcnt vmcnt(6) lgkmcnt(0)\n"
      "s_barrier\n"
      "v_mfma_f32_16x16x32_bf16 v[64:67], v[112:115], v[208:211], v[64:67]\n"
      "ds_read_b64_tr_b16 v[96:97], v42 offset:8192\n"
      "ds_read_b64_tr_b16 v[98:99], v42 offset:8704\n"
      "s_mul_i32 %[su], %[ck], 0x30000\n"
      "s_add_u32 m0, %[ml], 24576\n"
      "s_add_u32 %[ck], %[ck], 1\n"
      "buffer_load_dwordx4 %[lw], %[rw], %[su] offen lds\n"
      "v_mfma_f32_16x16x32_bf16 v[68:71], v[112:115], v[212:215], v[68:71]\n"
      "ds_read_b128 v[224:227], v36 offset:0\n"
      "ds_read_b128 v[228:231], v37 offset:0\n"
      "v_mfma_f32_16x16x32_bf16 v[48:51], v[112:115], v[92:95], v[48:51]\n"
      "ds_read_b64_tr_b16 v[104:105], v42 offset:16384\n"
      "ds_read_b64_tr_b16 v[106:107], v42 offset:16896\n"
      "v_lshlrev_b32 v62, 16, v208\n"
      "v_and_b32 v63, 0xffff0000, v208\n"
      "v_add_f32 v60, v60, v62\n"
      "v_mfma_f32_16x16x32_bf16 v[72:75], v[208:211], v[120:123], v[72:75]\n"
      "ds_read_b64_tr_b16 v[100:101], v43 offset:8192\n"
      "ds_read_b64_tr_b16 v[102:103], v43 offset:8704\n"
      "v_fmac_f32 v61, v62, v62\n"
      "v_add_f32 v60, v60, v63\n"
      "v_fmac_f32 v61, v63, v63\n"
      "s_and_b32 %[ck], %[ck], 7\n"
      "s_mul_i32 %[su], %[ck], 0x30000\n"
      "s_add_u32 m0, %[ml], 32768\n"
      "s_add_u32 %[ck], %[ck], 1\n"
      "buffer_load_dwordx4 %[lw], %[rw], %[su] offen lds\n"
      "v_mfma_f32_16x16x32_bf16 v[76:79], v[212:215], v[120:123], v[76:79]\n"
      "ds_read_b128 v[232:235], v38 offset:0\n"
      "ds_read_b128 v[236:239], v39 offset:0\n"
      "v_lshlrev_b32 v62, 16, v209\n"
      "v_and_b32 v63, 0xffff0000, v209\n"
      "v_add_f32 v60, v60, v62\n"
      "v_mfma_f32_16x16x32_bf16 v[52:55], v[92:95], v[120:123], v[52:55]\n"
      "ds_read_b64_tr_b16 v[108:109], v43 offset:16384\n"
      "ds_read_b64_tr_b16 v[110:111], v43 offset:16896\n"
      "v_fmac_f32 v61, v62, v62\n"
      "v_add_f32 v60, v60, v63\n"
      "v_fmac_f32 v61, v63, v63\n"
      "v_mfma_f32_16x16x32_bf16 v[64:67], v[116:119], v[216:219], v[64:67]\n"
      "v_lshlrev_b32 v62, 16, v210\n"
      "v_and_b32 v63, 0xffff0000, v210\n"
      "v_add_f32 v60, v60, v62\n"
      "s_and_b32 %[ck], %[ck], 7\n"
      "s_mul_i32 %[su], %[ck], 0x30000\n"
      "s_add_u32 m0, %[ml], 40960\n"
      "s_add_u32 %[ck], %[ck], 1\n"
      "buffer_load_dwordx4 %[lw], %[rw], %[su] offen lds\n"
      "s_and_b32 %[ck], %[ck], 7\n"
      "v_mfma_f32_16x16x32_bf16 v[68:71], v[116:119], v[220:223], v[68:71]\n"
      "v_fmac_f32 v61, v62, v62\n"
      "v_add_f32 v60, v60, v63\n"
      "v_fmac_f32 v61, v63, v63\n"
      "v_mfma_f32_16x16x32_bf16 v[72:75], v[216:219], v[124:127], v[72:75]\n"
      "v_lshlrev_b32 v62, 16, v211\n"
      "v_and_b32 v63, 0xffff0000, v211\n"
      "v_add_f32 v60, v60, v62\n"
      "v_mfma_f32_16x16x32_bf16 v[76:79], v[220:223], v[124:127], v[76:79]\n"
      "v_fmac_f32 v61, v62, v62\n"
      "v_add_f32 v60, v60, v63\n"
      "v_fmac_f32 v61, v63, v63\n"
      "s_waitcnt vmcnt(6) lgkmcnt(0)\n"
      "s_barrier\n"
      "v_mfma_f32_16x16x32_bf16 v[64:67], v[96:99], v[224:227], v[64:67]\n"
      "ds_read_b64_tr_b16 v[112:113], v42 offset:32768\n"
      "ds_read_b64_tr_b16 v[114:115], v42 offset:33280\n"
      "s_mul_i32 %[su], %[ck], 0x30000\n"
      "s_add_u32 m0, %[ml], 49152\n"
      "s_add_u32 %[ck], %[ck], 1\n"
      "buffer_load_dwordx4 %[lw], %[rw], %[su] offen lds\n"
      "v_mfma_f32_16x16x32_bf16 v[68:71], v[96:99], v[228:231], v[68:71]\n"
      "ds_read_b128 v[240:243], v36 offset:24576\n"
      "ds_read_b128 v[244:247], v37 offset:24576\n"
      "v_mfma_f32_16x16x32_bf16 v[48:51], v[96:99], v[92:95], v[48:51]\n"
      "ds_read_b64_tr_b16 v[120:121], v42 offset:40960\n"
      "ds_read_b64_tr_b16 v[122:123], v42 offset:41472\n"
      "v_lshlrev_b32 v62, 16, v224\n"
      "v_and_b32 v63, 0xffff0000, v224\n"
      "v_add_f32 v60, v60, v62\n"
      "v_mfma_f32_16x16x32_bf16 v[72:75], v[224:227], v[104:107], v[72:75]\n"
      "ds_read_b64_tr_b16 v[116:117], v43 offset:32768\n"
      "ds_read_b64_tr_b16 v[118:119], v43 offset:33280\n"
      "v_fmac_f32 v61, v62, v62\n"
      "v_add_f32 v60, v60, v63\n"
      "v_fmac_f32 v61, v63, v63\n"
      "s_and_b32 %[ck], %[ck], 7\n"
      "s_mul_i32 %[su], %[ck], 0x30000\n"
      "s_add_u32 m0, %[ml], 57344\n"
      "s_add_u32 %[ck], %[ck], 1\n"
      "buffer_load_dwordx4 %[lw], %[rw], %[su] offen lds\n"
      "s_and_b32 %[ck], %[ck], 7\n"
      "v_mfma_f32_16x16x32_bf16 v[76:79], v[228:231], v[104:107], v[76:79]\n"
      "ds_read_b128 v[248:251], v38 offset:24576\n"
      "ds_read_b128 v[252:255], v39 offset:24576\n"
      "v_lshlrev_b32 v62, 16, v225\n"
      "v_and_b32 v63, 0xffff0000, v225\n"
      "v_add_f32 v60, v60, v62\n"
      "v_mfma_f32_16x16x32_bf16 v[52:55], v[92:95], v[104:107], v[52:55]\n"
      "ds_read_b64_tr_b16 v[124:125], v43 offset:40960\n"
      "ds_read_b64_tr_b16 v[126:127], v43 offset:41472\n"
      "v_fmac_f32 v61, v62, v62\n"
      "v_add_f32 v60, v60, v63\n"
      "v_fmac_f32 v61, v63, v63\n"
      "v_mfma_f32_16x16x32_bf16 v[64:67], v[100:103], v[232:235], v[64:67]\n"
      "v_lshlrev_b32 v62, 16, v226\n"
      "v_and_b32 v63, 0xffff0000, v226\n"
      "v_add_f32 v60, v60, v62\n"
      "v_mfma_f32_16x16x32_bf16 v[68:71], v[100:103], v[236:239], v[68:71]\n"
      "v_fmac_f32 v61, v62, v62\n"
      "v_add_f32 v60, v60, v63\n"
      "v_fmac_f32 v61, v63, v63\n"
      "v_mfma_f32_16x16x32_bf16 v[72:75], v[232:235], v[108:111], v[72:75]\n"
      "v_lshlrev_b32 v62, 16, v227\n"
      "v_and_b32 v63, 0xffff0000, v227\n"
      "v_add_f32 v60, v60, v62\n"
      "v_mfma_f32_16x16x32_bf16 v[76:79], v[236:239], v[108:111], v[76:79]\n"
      "v_fmac_f32 v61, v62, v62\n"
      "v_add_f32 v60, v60, v63\n"
      "v_fmac_f32 v61, v63, v63\n"
      "s_waitcnt lgkmcnt(0)\n"
      "v_mfma_f32_16x16x32_bf16 v[64:67], v[112:115], v[240:243], v[64:67]\n"
      "v_lshlrev_b32 v62, 16, v240\n"
      "v_and_b32 v63, 0xffff0000, v240\n"
      "v_add_f32 v60, v60, v62\n"
      "v_mfma_f32_16x16x32_bf16 v[68:71], v[112:115], v[244:247], v[68:71]\n"
      "v_fmac_f32 v61, v62, v62\n"
      "v_add_f32 v60, v60, v63\n"
      "v_fmac_f32 v61, v63, v63\n"
      "v_mfma_f32_16x16x32_bf16 v[48:51], v[112:115], v[92:95], v[48:51]\n"
      "v_lshlrev_b32 v62, 16, v241\n"
      "v_and_b32 v63, 0xffff0000, v241\n"
      "v_add_f32 v60, v60, v62\n"
      "v_mfma_f32_16x16x32_bf16 v[72:75], v[240:243], v[120:123], v[72:75]\n"
      "v_fmac_f32 v61, v62, v62\n"
      "v_add_f32 v60, v60, v63\n"
      "v_fmac_f32 v61, v63, v63\n"
      "v_mfma_f32_16x16x32_bf16 v[76:79], v[244:247], v[120:123], v[76:79]\n"
      "v_lshlrev_b32 v62, 16, v242\n"
      "v_and_b32 v63, 0xffff0000, v242\n"
      "v_add_f32 v60, v60, v62\n"
      "v_mfma_f32_16x16x32_bf16 v[52:55], v[92:95], v[120:123], v[52:55]\n"
      "v_fmac_f32 v61, v62, v62\n"
      "v_add_f32 v60, v60, v63\n"
      "v_fmac_f32 v61, v63, v63\n"
      "v_mfma_f32_16x16x32_bf16 v[64:67], v[116:119], v[248:251], v[64:67]\n"
      "v_lshlrev_b32 v62, 16, v243\n"
      "v_and_b32 v63, 0xffff0000, v243\n"
      "v_add_f32 v60, v60, v62\n"
      "v_mfma_f32_16x16x32_bf16 v[68:71], v[116:119], v[252:255], v[68:71]\n"
      "v_fmac_f32 v61, v62, v62\n"
      "v_add_f32 v60, v60, v63\n"
      "v_fmac_f32 v61, v63, v63\n"
      "v_mfma_f32_16x16x32_bf16 v[72:75], v[248:251], v[124:127], v[72:75]\n"
      "v_mfma_f32_16x16x32_bf16 v[76:79], v[252:255], v[124:127], v[76:79]\n"
      "s_nop 7\n"
        : "=&{v[64:67]}"(ak0), "=&{v[68:71]}"(ak1), "=&{v[72:75]}"(av0), "=&{v[76:79]}"(av1), "=&{v[48:51]}"(csk), "=&{v[52:55]}"(csv),
          "=&{v[60:61]}"(st2), "=&{v[92:95]}"(one_r), "=&{v[128:143]}"(x0), "=&{v[144:159]}"(x1), "=&{v[160:175]}"(x2),
          "=&{v[176:191]}"(x3), "=&{v[192:207]}"(x4), "=&{v[208:223]}"(x5), "=&{v[224:239]}"(x6), "=&{v[240:255]}"(x7), [st] "=&s"(st),
          [su] "=&s"(su), [ck] "+s"(ck), "=&{v[88:89]}"(epoch), "=&{s[52:67]}"(rw4), [rwr] "=&s"(rwr), [rfl] "=&s"(rfl), [rf0] "=&s"(rf0),
          [rf1] "=&s"(rf1), [po] "=&s"(pout)
        : "{v[32:35]}"(xa), "{v[36:39]}"(xb), "{v[40:43]}"(wa), [lx] "v"(lx), [lw] "v"(lw), [rx] "s"(rx), [rw] "s"(rw), [ml] "s"(ml),
          [rws] "s"(rws), "{s68}"(d0), [dt] "s"(dt), [ifl] "s"(16 * (w == 0 ? D_WS : D_NULL)),
          [if0] "s"(16 * (role_gu ? D_WG : role_dn ? D_WD : D_NULL)), [if1] "s"(16 * (role_gu ? D_WU : role_dn ? D_WD : D_NULL))
        : "memory", "scc",
        "v62", "v63", "v96", "v97", "v98", "v99", "v100", "v101", "v102", "v103", "v104", "v105",
        "v106", "v107", "v108", "v109", "v110", "v111", "v112", "v113", "v114", "v115", "v116", "v117",
        "v118", "v119", "v120", "v121", "v122", "v123", "v124", "v125", "v126", "v127"
    );
  }
  const u64 tag = (((u64)uni((u32)(epoch >> 32)) << 32) | uni((u32)epoch)) * 6364136223846793005ull + 1442695040888963407ull;
  {
    const f32x2 v = {add32(add16(st2[0])), add32(add16(st2[1]))};
    *(LAS f32x2*)(L + e_w) = v;
    *(LAS f32x4*)(L + e_cs + th * 768) = csk;
    *(LAS float*)(L + e_cv + th * 768) = csv[0];
  }
  lds_barrier();
  __builtin_amdgcn_s_setprio(1);
  u32 want, tmp, rdy, cnt, dum;
  u64 ret;
  u32x2 rfl2;
  {
    const f32x2 ta = *(LAS f32x2*)(L + e_sp), tb = *(LAS f32x2*)(L + e_sp + 512);
    const f32x4 ca = *(LAS f32x4*)(L + e_cs), cb = *(LAS f32x4*)(L + e_cs + 768);
    const float cc = *(LAS float*)(L + e_cv), cd = *(LAS float*)(L + e_cv + 768);
    __builtin_amdgcn_sched_barrier(0);
    const f32x2 sc = ln_scalars(ta[0] + tb[0], ta[1] + tb[1]);
    *(LAS float*)(L + L_ST + w * 256 + lane * 4) = hh ? sc[1] : sc[0];
    f32x4 mt[2], rt[2];
#pragma unroll
    for (int r = 0; r < 2; ++r) {
      mt[r] = *(LAS f32x4*)(L + e_s4 + r * 64);
      rt[r] = *(LAS f32x4*)(L + e_s4 + r * 64 + 128);
    }
    __builtin_amdgcn_sched_barrier(0);
    const f32x4 ck4 = ca + cb;
    const float cv = cc + cd;
    auto m2 = __builtin_amdgcn_permlane16_swap(asu(sc[0]), asu(sc[0]), false, false);
    auto r2 = __builtin_amdgcn_permlane16_swap(asu(sc[1]), asu(sc[1]), false, false);
    const float mb[2] = {asf(m2[0]), asf(m2[1])}, rd[2] = {asf(r2[0]), asf(r2[1])};
#pragma unroll
    for (int r = 0; r < 2; ++r) {
      const f32x4 ak = r ? ak1 : ak0, av = r ? av1 : av0;
      float kv[4], vv[4];
#pragma unroll
      for (int i = 0; i < 4; ++i) {
        kv[i] = (ak[i] - mb[r] * ck4[i]) * rd[r];
        vv[i] = (av[i] - mt[r][i] * cv) * rt[r][i];
      }
      const u32x4 kvw = join16(u32x2{pack2(kv[0], kv[1]), pack2(kv[2], kv[3])}, u32x2{pack2(vv[0], vv[1]), pack2(vv[2], vv[3])});
      const u32 inner = r ? e_in ^ (256 << (g & 1)) : e_in;
      __builtin_amdgcn_raw_buffer_store_b128(kvw, rs, inner, WS_KV + ((4 * hg + j) * 64 + 2 * tg + th) * 2048, AUX_SC1);
      asm volatile("s_nop 1" ::"v"(kvw));
      sts128(L, L_OWN + (j * 2 + th) * 2048 + inner, kvw);
    }
    VMWAIT(2);
    lds_barrier();
    asm volatile(
      "s_waitcnt lgkmcnt(0)\n"
      "ds_read_b64_tr_b16 v[96:97], v40 offset:0\n"
      "ds_read_b64_tr_b16 v[98:99], v40 offset:512\n"
      "ds_read_b64_tr_b16 v[100:101], v41 offset:0\n"
      "ds_read_b64_tr_b16 v[102:103], v41 offset:512\n"
      "ds_read_b64_tr_b16 v[104:105], v40 offset:8192\n"
      "ds_read_b64_tr_b16 v[106:107], v40 offset:8704\n"
      "ds_read_b64_tr_b16 v[108:109], v41 offset:8192\n"
      "ds_read_b64_tr_b16 v[110:111], v41 offset:8704\n"
      "ds_read_b64_tr_b16 v[112:113], v40 offset:16384\n"
      "ds_read_b64_tr_b16 v[114:115], v40 offset:16896\n"
      "ds_read_b64_tr_b16 v[116:117], v41 offset:16384\n"
      "ds_read_b64_tr_b16 v[118:119], v41 offset:16896\n"
      "v_mov_b64 v[34:35], %[tag]\n"
      "v_mov_b32 v36, 1\n"
      "s_waitcnt lgkmcnt(10)\n"
      "v_mfma_f32_16x16x32_bf16 v[80:83], v[96:99], v[128:131], 0\n"
      "ds_read_b64_tr_b16 v[120:121], v40 offset:24576\n"
      "ds_read_b64_tr_b16 v[122:123], v40 offset:25088\n"
      "v_mfma_f32_16x16x32_bf16 v[84:87], v[96:99], v[132:135], 0\n"
      "v_mfma_f32_16x16x32_bf16 v[56:59], v[96:99], v[92:95], 0\n"
      "s_waitcnt vmcnt(0)\n"
      "s_mov_b64 exec, 1\n"
      "ds_add_rtn_u32 v37, v27, v36 offset:4\n"
      "s_mov_b64 exec, -1\n"
      "s_waitcnt lgkmcnt(11)\n"
      "v_mfma_f32_16x16x32_bf16 v[80:83], v[100:103], v[136:139], v[80:83]\n"
      "ds_read_b64_tr_b16 v[124:125], v41 offset:24576\n"
      "ds_read_b64_tr_b16 v[126:127], v41 offset:25088\n"
      "v_mfma_f32_16x16x32_bf16 v[84:87], v[100:103], v[140:143], v[84:87]\n"
      "v_mfma_f32_16x16x32_bf16 v[56:59], v[100:103], v[92:95], v[56:59]\n"
      "s_waitcnt lgkmcnt(2)\n"
      "v_readfirstlane_b32 %[tmp], v37\n"
      "s_cmp_lg_u32 %[tmp], 7\n"
      "s_cbranch_scc1 2f\n"
      "s_mov_b64 exec, 1\n"
      "buffer_store_dwordx2 v[34:35], off, %[rws], %[sfl] sc1\n"
      "s_mov_b64 exec, -1\n"
      "2:\n"
      "s_setprio 0\n"
      "s_waitcnt lgkmcnt(11)\n"
      "v_mfma_f32_16x16x32_bf16 v[80:83], v[104:107], v[144:147], v[80:83]\n"
      "ds_read_b64_tr_b16 v[96:97], v40 offset:32768\n"
      "ds_read_b64_tr_b16 v[98:99], v40 offset:33280\n"
      "v_mfma_f32_16x16x32_bf16 v[84:87], v[104:107], v[148:151], v[84:87]\n"
      "v_mfma_f32_16x16x32_bf16 v[56:59], v[104:107], v[92:95], v[56:59]\n"
      "s_waitcnt lgkmcnt(11)\n"
      "v_mfma_f32_16x16x32_bf16 v[80:83], v[108:111], v[152:155], v[80:83]\n"
      "ds_read_b64_tr_b16 v[100:101], v41 offset:32768\n"
      "ds_read_b64_tr_b16 v[102:103], v41 offset:33280\n"
      "v_mfma_f32_16x16x32_bf16 v[84:87], v[108:111], v[156:159], v[84:87]\n"
      "v_mfma_f32_16x16x32_bf16 v[56:59], v[108:111], v[92:95], v[56:59]\n"
      "s_waitcnt lgkmcnt(11)\n"
      "v_mfma_f32_16x16x32_bf16 v[80:83], v[112:115], v[160:163], v[80:83]\n"
      "ds_read_b64_tr_b16 v[104:105], v40 offset:40960\n"
      "ds_read_b64_tr_b16 v[106:107], v40 offset:41472\n"
      "v_mfma_f32_16x16x32_bf16 v[84:87], v[112:115], v[164:167], v[84:87]\n"
      "v_mfma_f32_16x16x32_bf16 v[56:59], v[112:115], v[92:95], v[56:59]\n"
      "s_waitcnt lgkmcnt(11)\n"
      "v_mfma_f32_16x16x32_bf16 v[80:83], v[116:119], v[168:171], v[80:83]\n"
      "ds_read_b64_tr_b16 v[108:109], v41 offset:40960\n"
      "ds_read_b64_tr_b16 v[110:111], v41 offset:41472\n"
      "v_mfma_f32_16x16x32_bf16 v[84:87], v[116:119], v[172:175], v[84:87]\n"
      "v_mfma_f32_16x16x32_bf16 v[56:59], v[116:119], v[92:95], v[56:59]\n"
      "s_waitcnt lgkmcnt(11)\n"
      "v_mfma_f32_16x16x32_bf16 v[80:83], v[120:123], v[176:179], v[80:83]\n"
      "ds_read_b64_tr_b16 v[112:113], v42 offset:0\n"
      "ds_read_b64_tr_b16 v[114:115], v42 offset:512\n"
      "v_mfma_f32_16x16x32_bf16 v[84:87], v[120:123], v[180:183], v[84:87]\n"
      "v_mfma_f32_16x16x32_bf16 v[56:59], v[120:123], v[92:95], v[56:59]\n"
      "s_waitcnt lgkmcnt(10)\n"
      "v_mfma_f32_16x16x32_bf16 v[80:83], v[124:127], v[184:187], v[80:83]\n"
      "ds_read_b64_tr_b16 v[116:117], v43 offset:0\n"
      "ds_read_b64_tr_b16 v[118:119], v43 offset:512\n"
      "v_mfma_f32_16x16x32_bf16 v[84:87], v[124:127], v[188:191], v[84:87]\n"
      "v_mfma_f32_16x16x32_bf16 v[56:59], v[124:127], v[92:95], v[56:59]\n"
      "s_waitcnt lgkmcnt(10)\n"
      "v_mfma_f32_16x16x32_bf16 v[80:83], v[96:99], v[192:195], v[80:83]\n"
      "ds_read_b64_tr_b16 v[120:121], v42 offset:8192\n"
      "ds_read_b64_tr_b16 v[122:123], v42 offset:8704\n"
      "v_mfma_f32_16x16x32_bf16 v[84:87], v[96:99], v[196:199], v[84:87]\n"
      "v_mfma_f32_16x16x32_bf16 v[56:59], v[96:99], v[92:95], v[56:59]\n"
      "s_waitcnt lgkmcnt(10)\n"
      "v_mfma_f32_16x16x32_bf16 v[80:83], v[100:103], v[200:203], v[80:83]\n"
      "ds_read_b64_tr_b16 v[124:125], v43 offset:8192\n"
      "ds_read_b64_tr_b16 v[126:127], v43 offset:8704\n"
      "v_mfma_f32_16x16x32_bf16 v[84:87], v[100:103], v[204:207], v[84:87]\n"
      "v_mfma_f32_16x16x32_bf16 v[56:59], v[100:103], v[92:95], v[56:59]\n"
      "s_waitcnt lgkmcnt(10)\n"
      "v_mfma_f32_16x16x32_bf16 v[80:83], v[104:107], v[208:211], v[80:83]\n"
      "v_mfma_f32_16x16x32_bf16 v[84:87], v[104:107], v[212:215], v[84:87]\n"
      "v_mfma_f32_16x16x32_bf16 v[56:59], v[104:107], v[92:95], v[56:59]\n"
      "s_waitcnt lgkmcnt(8)\n"
      "v_mfma_f32_16x16x32_bf16 v[80:83], v[108:111], v[216:219], v[80:83]\n"
      "v_mfma_f32_16x16x32_bf16 v[84:87], v[108:111], v[220:223], v[84:87]\n"
      "v_mfma_f32_16x16x32_bf16 v[56:59], v[108:111], v[92:95], v[56:59]\n"
      "s_waitcnt lgkmcnt(6)\n"
      "v_mfma_f32_16x16x32_bf16 v[80:83], v[112:115], v[224:227], v[80:83]\n"
      "v_mfma_f32_16x16x32_bf16 v[84:87], v[112:115], v[228:231], v[84:87]\n"
      "v_mfma_f32_16x16x32_bf16 v[56:59], v[112:115], v[92:95], v[56:59]\n"
      "s_waitcnt lgkmcnt(4)\n"
      "v_mfma_f32_16x16x32_bf16 v[80:83], v[116:119], v[232:235], v[80:83]\n"
      "v_mfma_f32_16x16x32_bf16 v[84:87], v[116:119], v[236:239], v[84:87]\n"
      "v_mfma_f32_16x16x32_bf16 v[56:59], v[116:119], v[92:95], v[56:59]\n"
      "s_waitcnt lgkmcnt(2)\n"
      "v_mfma_f32_16x16x32_bf16 v[80:83], v[120:123], v[240:243], v[80:83]\n"
      "v_mfma_f32_16x16x32_bf16 v[84:87], v[120:123], v[244:247], v[84:87]\n"
      "v_mfma_f32_16x16x32_bf16 v[56:59], v[120:123], v[92:95], v[56:59]\n"
      "s_waitcnt lgkmcnt(0)\n"
      "v_mfma_f32_16x16x32_bf16 v[80:83], v[124:127], v[248:251], v[80:83]\n"
      "v_mfma_f32_16x16x32_bf16 v[84:87], v[124:127], v[252:255], v[84:87]\n"
      "v_mfma_f32_16x16x32_bf16 v[56:59], v[124:127], v[92:95], v[56:59]\n"
      "s_nop 7\n"
        : "=&{v[80:83]}"(aq0), "=&{v[84:87]}"(aq1), "=&{v[56:59]}"(csq), [tmp] "=&s"(tmp)
        : "{v[128:143]}"(x0), "{v[144:159]}"(x1), "{v[160:175]}"(x2), "{v[176:191]}"(x3), "{v[192:207]}"(x4), "{v[208:223]}"(x5),
          "{v[224:239]}"(x6), "{v[240:255]}"(x7), "{v[40:43]}"(wa), "{v[92:95]}"(one_r), "{v[24:27]}"(va2), [rws] "s"(rws),
          [tag] "s"(tag), [sfl] "s"(WS_FLG + (hg * 32 + tg) * FSTR)
        : "memory", "scc",
        "v34", "v35", "v36", "v37", "v96", "v97", "v98", "v99", "v100", "v101", "v102", "v103",
        "v104", "v105", "v106", "v107", "v108", "v109", "v110", "v111", "v112", "v113", "v114", "v115",
        "v116", "v117", "v118", "v119", "v120", "v121", "v122", "v123", "v124", "v125", "v126", "v127"
    );
#pragma unroll
    for (int r = 0; r < 2; ++r) {
      const float rq = rd[r] * (0.25f * 1.44269504088896340736f);
      const f32x4 aq = r ? aq1 : aq0;
      float qv[4];
#pragma unroll
      for (int i = 0; i < 4; ++i) qv[i] = (aq[i] - mb[r] * csq[i]) * rq;
      *(LAS u32x2*)(L + L_QX + (j * 2 + th) * 1024 + hh * 512 + (16 * (r ^ tx) + l15) * 16 + (g & 1) * 8) =
          u32x2{pack2(qv[0], qv[1]), pack2(qv[2], qv[3])};
    }
  }
  asm volatile("s_cmp_lg_u32 %3, 0\n\ts_cbranch_scc1 2f\n\ts_nop 2\n\tbuffer_load_dwordx2 v[32:33], v25, %2, 0 offen sc1\n2:"
               : "={v[32:33]}"(rfl2) : "{v[24:27]}"(va2), "s"(rws), "s"(w) : "memory", "scc");
  lds_barrier();

  const u32x4 qa = lds128(L, L_QX + (j * 2 + (th ^ 1)) * 1024 + lane * 16), qb = lds128(L, L_QX + (j * 2 + th) * 1024 + lane * 16);
  f32x16 o;
  u32x16 ffa, ffb;
  u32x2 xres0, xres1;
  u32x4 wr4;
  {
    const u32x4 k0 = lds128(L, L_OWN + j * 4096 + lane * 16);
    u32x4 rk = lds128(L, L_OWN + (j * 2 + th) * 2048 + lane * 16);
    u32x4 rv0 = lds128(L, L_OWN + (j * 2 + th) * 2048 + 1024 + hh * 256 + l15 * 16);
    u32x4 rv1 = lds128(L, L_OWN + (j * 2 + th) * 2048 + 1536 + hh * 256 + l15 * 16);
    if (w == 0) {
      asm volatile("s_waitcnt vmcnt(0)" : "+{v[32:33]}"(rfl2)::"memory");
      const u32 m = (u32)__ballot((rfl2.x == (u32)tag) & (rfl2.y == (u32)(tag >> 32)));
      *(LAS u32*)(L + L_RDY) = m;
    }
    lds_barrier();
    rdy = uni(*(LAS u32*)(L + L_RDY));
    if (lane & 16) rv0 = rv1 = u32x4{va2[0], va2[0], va2[0], va2[0]};
    u32 wi = xi >> 2;
    asm volatile(
      "s_branch 8f\n"
      "9:\n"
      "s_mov_b32 %[cnt], 0x20000\n"
      "7:\n"
      "s_cmp_lg_u32 %[wid], 0\n"
      "s_cbranch_scc1 5f\n"
      "buffer_load_dwordx2 v[32:33], v25, %[rws], 0 offen sc1\n"
      "s_waitcnt vmcnt(0)\n"
      "v_cmp_eq_u64 vcc, %[tag], v[32:33]\n"
      "s_mov_b32 %[rdy], vcc_lo\n"
      "v_mov_b32 v32, %[rdy]\n"
      "ds_or_b32 v27, v32\n"
      "s_branch 4f\n"
      "5:\n"
      "ds_read_b32 v32, v27\n"
      "s_waitcnt lgkmcnt(0)\n"
      "v_readfirstlane_b32 %[rdy], v32\n"
      "4:\n"
      "s_andn2_b32 %[tmp], %[want], %[rdy]\n"
      "s_cbranch_scc0 6f\n"
      "s_sleep 2\n"
      "s_sub_u32 %[cnt], %[cnt], 1\n"
      "s_cbranch_scc0 7b\n"
      "6:\n"
      "s_setpc_b64 %[ret]\n"
      "8:\n"
      "s_setprio 3\n"
      "v_mov_b32 v248, 0\n"
      "v_mov_b32 v249, 0\n"
      "v_mov_b32 v250, 0\n"
      "v_mov_b32 v251, 0\n"
      "v_mfma_f32_32x32x16_bf16 v[208:223], v[252:255], v[240:243], 0\n"
      "v_mfma_f32_32x32x16_bf16 v[224:239], v[252:255], v[244:247], 0\n"
      "v_mov_b32 v56, v24\n"
      "v_mov_b32 v57, v24\n"
      "v_mov_b32 v58, v24\n"
      "v_mov_b32 v59, v24\n"
      "v_mov_b32 v60, v24\n"
      "v_mov_b32 v61, v24\n"
      "v_mov_b32 v62, v24\n"
      "v_mov_b32 v63, v24\n"
      "v_mfma_f32_32x32x16_bf16 v[144:159], v[248:251], v[248:251], 0\n"
      "v_mov_b32 v80, v24\n"
      "v_mov_b32 v81, v24\n"
      "v_mov_b32 v82, v24\n"
      "v_mov_b32 v83, v24\n"
      "v_mov_b32 v84, v24\n"
      "v_mov_b32 v85, v24\n"
      "v_mov_b32 v86, v24\n"
      "v_mov_b32 v87, v24\n"
      "v_mfma_f32_32x32x16_bf16 v[160:175], v[248:251], v[248:251], 0\n"
      "s_andn2_b32 %[dum], 6, %[rdy]\n"
      "s_cbranch_scc1 1f\n"
      "buffer_load_dwordx4 v[52:55], v16, %[rws], %[so] offen sc1\n"
      "s_mov_b64 exec, %[msk]\n"
      "buffer_load_dwordx4 v[56:59], v17, %[rws], %[so] offen offset:1024 sc1\n"
      "buffer_load_dwordx4 v[60:63], v17, %[rws], %[so] offen offset:1536 sc1\n"
      "s_mov_b64 exec, -1\n"
      "s_add_u32 %[so], %[so], 4096\n"
      "s_and_b32 %[so], %[so], 0x1ffff\n"
      "buffer_load_dwordx4 v[76:79], v16, %[rws], %[so] offen sc1\n"
      "s_mov_b64 exec, %[msk]\n"
      "buffer_load_dwordx4 v[80:83], v17, %[rws], %[so] offen offset:1024 sc1\n"
      "buffer_load_dwordx4 v[84:87], v17, %[rws], %[so] offen offset:1536 sc1\n"
      "s_mov_b64 exec, -1\n"
      "s_add_u32 %[so], %[so], 4096\n"
      "s_and_b32 %[so], %[so], 0x1ffff\n"
      "s_branch 2f\n"
      "1:\n"
      "buffer_load_dwordx2 v[34:35], v25, %[rfl], 0 offen sc1\n"
      "s_nop 3\n"
      "2:\n"
      "v_max3_f32 v208, v208, v209, v210\n"
      "v_max3_f32 v211, v211, v212, v213\n"
      "v_max3_f32 v214, v214, v215, v216\n"
      "v_max3_f32 v217, v217, v218, v219\n"
      "v_max3_f32 v220, v220, v221, v222\n"
      "v_max3_f32 v208, v208, v211, v214\n"
      "v_max3_f32 v217, v217, v220, v223\n"
      "v_max_f32 v208, v208, v217\n"
      "v_mov_b32 v209, v208\n"
      "s_nop 1\n"
      "v_permlane32_swap_b32 v208, v209\n"
      "v_min_f32 v176, -v208, -v209\n"
      "v_mov_b32 v177, v176\n"
      "v_mov_b32 v178, v176\n"
      "v_mov_b32 v179, v176\n"
      "v_mov_b32 v180, v176\n"
      "v_mov_b32 v181, v176\n"
      "v_mov_b32 v182, v176\n"
      "v_mov_b32 v183, v176\n"
      "v_mov_b32 v184, v176\n"
      "v_mov_b32 v185, v176\n"
      "v_mov_b32 v186, v176\n"
      "v_mov_b32 v187, v176\n"
      "v_mov_b32 v188, v176\n"
      "v_mov_b32 v189, v176\n"
      "v_mov_b32 v190, v176\n"
      "v_mov_b32 v191, v176\n"
      "s_nop 1\n"
      "v_mfma_f32_32x32x16_bf16 v[208:223], v[40:43], v[240:243], v[176:191]\n"
      "v_max3_f32 v224, v224, v225, v226\n"
      "v_max3_f32 v227, v227, v228, v229\n"
      "v_max3_f32 v230, v230, v231, v232\n"
      "v_max3_f32 v233, v233, v234, v235\n"
      "v_max3_f32 v236, v236, v237, v238\n"
      "v_max3_f32 v224, v224, v227, v230\n"
      "v_max3_f32 v233, v233, v236, v239\n"
      "v_max_f32 v224, v224, v233\n"
      "v_mov_b32 v225, v224\n"
      "s_nop 1\n"
      "v_permlane32_swap_b32 v224, v225\n"
      "v_min_f32 v192, -v224, -v225\n"
      "v_mov_b32 v193, v192\n"
      "v_mov_b32 v194, v192\n"
      "v_mov_b32 v195, v192\n"
      "v_mov_b32 v196, v192\n"
      "v_mov_b32 v197, v192\n"
      "v_mov_b32 v198, v192\n"
      "v_mov_b32 v199, v192\n"
      "v_mov_b32 v200, v192\n"
      "v_mov_b32 v201, v192\n"
      "v_mov_b32 v202, v192\n"
      "v_mov_b32 v203, v192\n"
      "v_mov_b32 v204, v192\n"
      "v_mov_b32 v205, v192\n"
      "v_mov_b32 v206, v192\n"
      "v_mov_b32 v207, v192\n"
      "v_mov_b32 v28, 0xdddddddd\n"
      "s_cmp_eq_u32 %[dum], 0\n"
      "s_cbranch_scc1 2f\n"
      "s_waitcnt vmcnt(0)\n"
      "v_cmp_eq_u64 vcc, %[tag], v[34:35]\n"
      "s_or_b32 %[rdy], %[rdy], vcc_lo\n"
      "v_mov_b32 v32, %[rdy]\n"
      "s_mov_b64 exec, 1\n"
      "ds_or_rtn_b32 v33, v27, v32\n"
      "s_mov_b64 exec, -1\n"
      "s_waitcnt lgkmcnt(0)\n"
      "v_readfirstlane_b32 %[tmp], v33\n"
      "s_or_b32 %[rdy], %[rdy], %[tmp]\n"
      "s_mov_b32 %[want], 0x2\n"
      "s_andn2_b32 %[tmp], %[want], %[rdy]\n"
      "s_cbranch_scc0 3f\n"
      "s_call_b64 %[ret], 9b\n"
      "3:\n"
      "buffer_load_dwordx4 v[52:55], v16, %[rws], %[so] offen sc1\n"
      "s_mov_b64 exec, %[msk]\n"
      "buffer_load_dwordx4 v[56:59], v17, %[rws], %[so] offen offset:1024 sc1\n"
      "buffer_load_dwordx4 v[60:63], v17, %[rws], %[so] offen offset:1536 sc1\n"
      "s_mov_b64 exec, -1\n"
      "s_add_u32 %[so], %[so], 4096\n"
      "s_and_b32 %[so], %[so], 0x1ffff\n"
      "s_mov_b32 %[want], 0x4\n"
      "s_andn2_b32 %[tmp], %[want], %[rdy]\n"
      "s_cbranch_scc0 3f\n"
      "s_call_b64 %[ret], 9b\n"
      "3:\n"
      "buffer_load_dwordx4 v[76:79], v16, %[rws], %[so] offen sc1\n"
      "s_mov_b64 exec, %[msk]\n"
      "buffer_load_dwordx4 v[80:83], v17, %[rws], %[so] offen offset:1024 sc1\n"
      "buffer_load_dwordx4 v[84:87], v17, %[rws], %[so] offen offset:1536 sc1\n"
      "s_mov_b64 exec, -1\n"
      "s_add_u32 %[so], %[so], 4096\n"
      "s_and_b32 %[so], %[so], 0x1ffff\n"
      "2:\n"
      "v_exp_f32 v208, v208\n"
      "v_exp_f32 v209, v209\n"
      "v_exp_f32 v210, v210\n"
      "v_exp_f32 v211, v211\n"
      "v_exp_f32 v212, v212\n"
      "v_mfma_f32_32x32x16_bf16 v[224:239], v[40:43], v[244:247], v[192:207]\n"
      "v_exp_f32 v213, v213\n"
      "v_exp_f32 v214, v214\n"
      "v_exp_f32 v215, v215\n"
      "v_exp_f32 v216, v216\n"
      "v_exp_f32 v217, v217\n"
      "v_exp_f32 v218, v218\n"
      "v_smfmac_f32_32x32x32_bf16 v[144:159], v[44:47], v[208:215], v28\n"
      "v_exp_f32 v219, v219\n"
      "v_exp_f32 v220, v220\n"
      "v_exp_f32 v221, v221\n"
      "v_exp_f32 v222, v222\n"
      "v_exp_f32 v223, v223\n"
      "v_exp_f32 v224, v224\n"
      "v_exp_f32 v225, v225\n"
      "v_smfmac_f32_32x32x32_bf16 v[144:159], v[48:51], v[216:223], v28\n"
      "v_exp_f32 v226, v226\n"
      "v_exp_f32 v227, v227\n"
      "v_exp_f32 v228, v228\n"
      "s_waitcnt vmcnt(3)\n"
      "v_mfma_f32_32x32x16_bf16 v[208:223], v[52:55], v[240:243], v[176:191]\n"
      "v_exp_f32 v229, v229\n"
      "v_exp_f32 v230, v230\n"
      "v_exp_f32 v231, v231\n"
      "v_exp_f32 v232, v232\n"
      "v_exp_f32 v233, v233\n"
      "v_exp_f32 v234, v234\n"
      "v_smfmac_f32_32x32x32_bf16 v[160:175], v[44:47], v[224:231], v28\n"
      "v_exp_f32 v235, v235\n"
      "v_exp_f32 v236, v236\n"
      "v_exp_f32 v237, v237\n"
      "v_exp_f32 v238, v238\n"
      "v_exp_f32 v239, v239\n"
      "v_exp_f32 v208, v208\n"
      "v_exp_f32 v209, v209\n"
      "v_smfmac_f32_32x32x32_bf16 v[160:175], v[48:51], v[232:239], v28\n"
      "s_mov_b32 %[want], 0x8\n"
      "s_andn2_b32 %[tmp], %[want], %[rdy]\n"
      "s_cbranch_scc0 3f\n"
      "s_call_b64 %[ret], 9b\n"
      "3:\n"
      "buffer_load_dwordx2 v[34:35], v25, %[rfl], 0 offen sc1\n"
      "v_mov_b32 v68, v24\n"
      "v_mov_b32 v69, v24\n"
      "v_mov_b32 v70, v24\n"
      "v_mov_b32 v71, v24\n"
      "v_mov_b32 v72, v24\n"
      "v_mov_b32 v73, v24\n"
      "v_mov_b32 v74, v24\n"
      "v_mov_b32 v75, v24\n"
      "buffer_load_dwordx4 v[64:67], v16, %[rws], %[so] offen sc1\n"
      "s_mov_b64 exec, %[msk]\n"
      "buffer_load_dwordx4 v[68:71], v17, %[rws], %[so] offen offset:1024 sc1\n"
      "buffer_load_dwordx4 v[72:75], v17, %[rws], %[so] offen offset:1536 sc1\n"
      "s_mov_b64 exec, -1\n"
      "s_add_u32 %[so], %[so], 4096\n"
      "s_and_b32 %[so], %[so], 0x1ffff\n"
      "v_exp_f32 v210, v210\n"
      "v_exp_f32 v211, v211\n"
      "v_exp_f32 v212, v212\n"
      "v_mfma_f32_32x32x16_bf16 v[224:239], v[52:55], v[244:247], v[192:207]\n"
      "v_exp_f32 v213, v213\n"
      "v_exp_f32 v214, v214\n"
      "v_exp_f32 v215, v215\n"
      "v_exp_f32 v216, v216\n"
      "v_exp_f32 v217, v217\n"
      "v_exp_f32 v218, v218\n"
      "v_smfmac_f32_32x32x32_bf16 v[144:159], v[56:59], v[208:215], v28\n"
      "s_mov_b32 %[want], 0x10\n"
      "s_andn2_b32 %[tmp], %[want], %[rdy]\n"
      "s_cbranch_scc0 3f\n"
      "s_call_b64 %[ret], 9b\n"
      "3:\n"
      "v_mov_b32 v92, v24\n"
      "v_mov_b32 v93, v24\n"
      "v_mov_b32 v94, v24\n"
      "v_mov_b32 v95, v24\n"
      "v_mov_b32 v96, v24\n"
      "v_mov_b32 v97, v24\n"
      "v_mov_b32 v98, v24\n"
      "v_mov_b32 v99, v24\n"
      "buffer_load_dwordx4 v[88:91], v16, %[rws], %[so] offen sc1\n"
      "s_mov_b64 exec, %[msk]\n"
      "buffer_load_dwordx4 v[92:95], v17, %[rws], %[so] offen offset:1024 sc1\n"
      "buffer_load_dwordx4 v[96:99], v17, %[rws], %[so] offen offset:1536 sc1\n"
      "s_mov_b64 exec, -1\n"
      "s_add_u32 %[so], %[so], 4096\n"
      "s_and_b32 %[so], %[so], 0x1ffff\n"
      "v_exp_f32 v219, v219\n"
      "v_exp_f32 v220, v220\n"
      "v_exp_f32 v221, v221\n"
      "v_exp_f32 v222, v222\n"
      "v_exp_f32 v223, v223\n"
      "v_exp_f32 v224, v224\n"
      "v_exp_f32 v225, v225\n"
      "v_smfmac_f32_32x32x32_bf16 v[144:159], v[60:63], v[216:223], v28\n"
      "v_exp_f32 v226, v226\n"
      "v_exp_f32 v227, v227\n"
      "v_exp_f32 v228, v228\n"
      "s_waitcnt vmcnt(7)\n"
      "v_mfma_f32_32x32x16_bf16 v[208:223], v[76:79], v[240:243], v[176:191]\n"
      "v_exp_f32 v229, v229\n"
      "v_exp_f32 v230, v230\n"
      "v_exp_f32 v231, v231\n"
      "v_exp_f32 v232, v232\n"
      "v_exp_f32 v233, v233\n"
      "v_exp_f32 v234, v234\n"
      "v_smfmac_f32_32x32x32_bf16 v[160:175], v[56:59], v[224:231], v28\n"
      "v_exp_f32 v235, v235\n"
      "v_exp_f32 v236, v236\n"
      "v_exp_f32 v237, v237\n"
      "v_exp_f32 v238, v238\n"
      "v_exp_f32 v239, v239\n"
      "v_exp_f32 v208, v208\n"
      "v_exp_f32 v209, v209\n"
      "v_smfmac_f32_32x32x32_bf16 v[160:175], v[60:63], v[232:239], v28\n"
      "s_mov_b32 %[want], 0x20\n"
      "s_andn2_b32 %[tmp], %[want], %[rdy]\n"
      "s_cbranch_scc0 3f\n"
      "s_call_b64 %[ret], 9b\n"
      "3:\n"
      "v_mov_b32 v104, v24\n"
      "v_mov_b32 v105, v24\n"
      "v_mov_b32 v106, v24\n"
      "v_mov_b32 v107, v24\n"
      "v_mov_b32 v108, v24\n"
      "v_mov_b32 v109, v24\n"
      "v_mov_b32 v110, v24\n"
      "v_mov_b32 v111, v24\n"
      "buffer_load_dwordx4 v[100:103], v16, %[rws], %[so] offen sc1\n"
      "s_mov_b64 exec, %[msk]\n"
      "buffer_load_dwordx4 v[104:107], v17, %[rws], %[so] offen offset:1024 sc1\n"
      "buffer_load_dwordx4 v[108:111], v17, %[rws], %[so] offen offset:1536 sc1\n"
      "s_mov_b64 exec, -1\n"
      "s_add_u32 %[so], %[so], 4096\n"
      "s_and_b32 %[so], %[so], 0x1ffff\n"
      "v_exp_f32 v210, v210\n"
      "v_exp_f32 v211, v211\n"
      "v_exp_f32 v212, v212\n"
      "v_mfma_f32_32x32x16_bf16 v[224:239], v[76:79], v[244:247], v[192:207]\n"
      "v_exp_f32 v213, v213\n"
      "v_exp_f32 v214, v214\n"
      "v_exp_f32 v215, v215\n"
      "v_exp_f32 v216, v216\n"
      "v_exp_f32 v217, v217\n"
      "v_exp_f32 v218, v218\n"
      "v_smfmac_f32_32x32x32_bf16 v[144:159], v[80:83], v[208:215], v28\n"
      "v_exp_f32 v219, v219\n"
      "v_exp_f32 v220, v220\n"
      "v_exp_f32 v221, v221\n"
      "v_exp_f32 v222, v222\n"
      "v_exp_f32 v223, v223\n"
      "v_exp_f32 v224, v224\n"
      "v_exp_f32 v225, v225\n"
      "v_smfmac_f32_32x32x32_bf16 v[144:159], v[84:87], v[216:223], v28\n"
      "v_exp_f32 v226, v226\n"
      "v_exp_f32 v227, v227\n"
      "v_exp_f32 v228, v228\n"
      "s_waitcnt vmcnt(6)\n"
      "v_mfma_f32_32x32x16_bf16 v[208:223], v[64:67], v[240:243], v[176:191]\n"
      "v_exp_f32 v229, v229\n"
      "v_exp_f32 v230, v230\n"
      "v_exp_f32 v231, v231\n"
      "v_exp_f32 v232, v232\n"
      "v_exp_f32 v233, v233\n"
      "v_exp_f32 v234, v234\n"
      "v_smfmac_f32_32x32x32_bf16 v[160:175], v[80:83], v[224:231], v28\n"
      "v_exp_f32 v235, v235\n"
      "v_exp_f32 v236, v236\n"
      "v_exp_f32 v237, v237\n"
      "v_exp_f32 v238, v238\n"
      "v_exp_f32 v239, v239\n"
      "v_exp_f32 v208, v208\n"
      "v_exp_f32 v209, v209\n"
      "v_smfmac_f32_32x32x32_bf16 v[160:175], v[84:87], v[232:239], v28\n"
      "s_waitcnt vmcnt(9)\n"
      "v_cmp_eq_u64 vcc, %[tag], v[34:35]\n"
      "s_or_b32 %[rdy], %[rdy], vcc_lo\n"
      "v_mov_b32 v32, %[rdy]\n"
      "s_mov_b64 exec, 1\n"
      "ds_or_rtn_b32 v33, v27, v32\n"
      "s_mov_b64 exec, -1\n"
      "s_waitcnt lgkmcnt(0)\n"
      "v_readfirstlane_b32 %[tmp], v33\n"
      "s_or_b32 %[rdy], %[rdy], %[tmp]\n"
      "s_mov_b32 %[want], 0xffffffff\n"
      "s_andn2_b32 %[tmp], %[want], %[rdy]\n"
      "s_cbranch_scc0 3f\n"
      "s_call_b64 %[ret], 9b\n"
      "3:\n"
      "buffer_load_dwordx4 v[40:43], v16, %[rws], %[so] offen sc1\n"
      "s_mov_b64 exec, %[msk]\n"
      "buffer_load_dwordx4 v[44:47], v17, %[rws], %[so] offen offset:1024 sc1\n"
      "buffer_load_dwordx4 v[48:51], v17, %[rws], %[so] offen offset:1536 sc1\n"
      "s_mov_b64 exec, -1\n"
      "s_add_u32 %[so], %[so], 4096\n"
      "s_and_b32 %[so], %[so], 0x1ffff\n"
      "v_exp_f32 v210, v210\n"
      "v_exp_f32 v211, v211\n"
      "v_exp_f32 v212, v212\n"
      "v_mfma_f32_32x32x16_bf16 v[224:239], v[64:67], v[244:247], v[192:207]\n"
      "v_exp_f32 v213, v213\n"
      "v_exp_f32 v214, v214\n"
      "v_exp_f32 v215, v215\n"
      "v_exp_f32 v216, v216\n"
      "v_exp_f32 v217, v217\n"
      "v_exp_f32 v218, v218\n"
      "v_smfmac_f32_32x32x32_bf16 v[144:159], v[68:71], v[208:215], v28\n"
      "buffer_load_dwordx4 v[52:55], v16, %[rws], %[so] offen sc1\n"
      "s_mov_b64 exec, %[msk]\n"
      "buffer_load_dwordx4 v[56:59], v17, %[rws], %[so] offen offset:1024 sc1\n"
      "buffer_load_dwordx4 v[60:63], v17, %[rws], %[so] offen offset:1536 sc1\n"
      "s_mov_b64 exec, -1\n"
      "s_add_u32 %[so], %[so], 4096\n"
      "s_and_b32 %[so], %[so], 0x1ffff\n"
      "s_buffer_load_dword %[dum], s[52:55], %[to0] offset:0\n"
      "v_exp_f32 v219, v219\n"
      "v_exp_f32 v220, v220\n"
      "v_exp_f32 v221, v221\n"
      "v_exp_f32 v222, v222\n"
      "v_exp_f32 v223, v223\n"
      "v_exp_f32 v224, v224\n"
      "v_exp_f32 v225, v225\n"
      "v_smfmac_f32_32x32x32_bf16 v[144:159], v[72:75], v[216:223], v28\n"
      "s_buffer_load_dword %[dum], s[52:55], %[to0] offset:1024\n"
      "v_exp_f32 v226, v226\n"
      "v_exp_f32 v227, v227\n"
      "v_exp_f32 v228, v228\n"
      "s_waitcnt vmcnt(9)\n"
      "v_mfma_f32_32x32x16_bf16 v[208:223], v[88:91], v[240:243], v[176:191]\n"
      "v_exp_f32 v229, v229\n"
      "v_exp_f32 v230, v230\n"
      "v_exp_f32 v231, v231\n"
      "v_exp_f32 v232, v232\n"
      "v_exp_f32 v233, v233\n"
      "v_exp_f32 v234, v234\n"
      "v_smfmac_f32_32x32x32_bf16 v[160:175], v[68:71], v[224:231], v28\n"
      "s_buffer_load_dword %[dum], s[52:55], %[to0] offset:2048\n"
      "v_exp_f32 v235, v235\n"
      "v_exp_f32 v236, v236\n"
      "v_exp_f32 v237, v237\n"
      "v_exp_f32 v238, v238\n"
      "v_exp_f32 v239, v239\n"
      "v_exp_f32 v208, v208\n"
      "v_exp_f32 v209, v209\n"
      "v_smfmac_f32_32x32x32_bf16 v[160:175], v[72:75], v[232:239], v28\n"
      "buffer_load_dwordx4 v[76:79], v16, %[rws], %[so] offen sc1\n"
      "s_mov_b64 exec, %[msk]\n"
      "buffer_load_dwordx4 v[80:83], v17, %[rws], %[so] offen offset:1024 sc1\n"
      "buffer_load_dwordx4 v[84:87], v17, %[rws], %[so] offen offset:1536 sc1\n"
      "s_mov_b64 exec, -1\n"
      "s_add_u32 %[so], %[so], 4096\n"
      "s_and_b32 %[so], %[so], 0x1ffff\n"
      "v_exp_f32 v210, v210\n"
      "v_exp_f32 v211, v211\n"
      "v_exp_f32 v212, v212\n"
      "v_mfma_f32_32x32x16_bf16 v[224:239], v[88:91], v[244:247], v[192:207]\n"
      "v_exp_f32 v213, v213\n"
      "v_exp_f32 v214, v214\n"
      "v_exp_f32 v215, v215\n"
      "v_exp_f32 v216, v216\n"
      "v_exp_f32 v217, v217\n"
      "v_exp_f32 v218, v218\n"
      "v_smfmac_f32_32x32x32_bf16 v[144:159], v[92:95], v[208:215], v28\n"
      "s_buffer_load_dword %[dum], s[52:55], %[to0] offset:3072\n"
      "v_exp_f32 v219, v219\n"
      "v_exp_f32 v220, v220\n"
      "v_exp_f32 v221, v221\n"
      "v_exp_f32 v222, v222\n"
      "v_exp_f32 v223, v223\n"
      "v_exp_f32 v224, v224\n"
      "v_exp_f32 v225, v225\n"
      "v_smfmac_f32_32x32x32_bf16 v[144:159], v[96:99], v[216:223], v28\n"
      "s_buffer_load_dword %[dum], s[52:55], %[to0] offset:4096\n"
      "v_exp_f32 v226, v226\n"
      "v_exp_f32 v227, v227\n"
      "v_exp_f32 v228, v228\n"
      "s_waitcnt vmcnt(9)\n"
      "v_mfma_f32_32x32x16_bf16 v[208:223], v[100:103], v[240:243], v[176:191]\n"
      "v_exp_f32 v229, v229\n"
      "v_exp_f32 v230, v230\n"
      "v_exp_f32 v231, v231\n"
      "v_exp_f32 v232, v232\n"
      "v_exp_f32 v233, v233\n"
      "v_exp_f32 v234, v234\n"
      "v_smfmac_f32_32x32x32_bf16 v[160:175], v[92:95], v[224:231], v28\n"
      "s_buffer_load_dword %[dum], s[52:55], %[to0] offset:5120\n"
      "v_exp_f32 v235, v235\n"
      "v_exp_f32 v236, v236\n"
      "v_exp_f32 v237, v237\n"
      "v_exp_f32 v238, v238\n"
      "v_exp_f32 v239, v239\n"
      "v_exp_f32 v208, v208\n"
      "v_exp_f32 v209, v209\n"
      "v_smfmac_f32_32x32x32_bf16 v[160:175], v[96:99], v[232:239], v28\n"
      "buffer_load_dwordx4 v[64:67], v16, %[rws], %[so] offen sc1\n"
      "s_mov_b64 exec, %[msk]\n"
      "buffer_load_dwordx4 v[68:71], v17, %[rws], %[so] offen offset:1024 sc1\n"
      "buffer_load_dwordx4 v[72:75], v17, %[rws], %[so] offen offset:1536 sc1\n"
      "s_mov_b64 exec, -1\n"
      "s_add_u32 %[so], %[so], 4096\n"
      "s_and_b32 %[so], %[so], 0x1ffff\n"
      "v_exp_f32 v210, v210\n"
      "v_exp_f32 v211, v211\n"
      "v_exp_f32 v212, v212\n"
      "v_mfma_f32_32x32x16_bf16 v[224:239], v[100:103], v[244:247], v[192:207]\n"
      "v_exp_f32 v213, v213\n"
      "v_exp_f32 v214, v214\n"
      "v_exp_f32 v215, v215\n"
      "v_exp_f32 v216, v216\n"
      "v_exp_f32 v217, v217\n"
      "v_exp_f32 v218, v218\n"
      "v_smfmac_f32_32x32x32_bf16 v[144:159], v[104:107], v[208:215], v28\n"
      "buffer_load_dwordx4 v[88:91], v16, %[rws], %[so] offen sc1\n"
      "s_mov_b64 exec, %[msk]\n"
      "buffer_load_dwordx4 v[92:95], v17, %[rws], %[so] offen offset:1024 sc1\n"
      "buffer_load_dwordx4 v[96:99], v17, %[rws], %[so] offen offset:1536 sc1\n"
      "s_mov_b64 exec, -1\n"
      "s_add_u32 %[so], %[so], 4096\n"
      "s_and_b32 %[so], %[so], 0x1ffff\n"
      "s_buffer_load_dword %[dum], s[52:55], %[to0] offset:6144\n"
      "v_exp_f32 v219, v219\n"
      "v_exp_f32 v220, v220\n"
      "v_exp_f32 v221, v221\n"
      "v_exp_f32 v222, v222\n"
      "v_exp_f32 v223, v223\n"
      "v_exp_f32 v224, v224\n"
      "v_exp_f32 v225, v225\n"
      "v_smfmac_f32_32x32x32_bf16 v[144:159], v[108:111], v[216:223], v28\n"
      "s_buffer_load_dword %[dum], s[52:55], %[to0] offset:7168\n"
      "v_mov_b32 v248, -1\n"
      "v_mov_b32 v249, -1\n"
      "v_mov_b32 v250, -1\n"
      "v_mov_b32 v251, -1\n"
      "buffer_store_dwordx4 v[248:251], v26, %[rws], %[sob] offen sc1\n"
      "v_exp_f32 v226, v226\n"
      "v_exp_f32 v227, v227\n"
      "v_exp_f32 v228, v228\n"
      "s_waitcnt vmcnt(13)\n"
      "v_mfma_f32_32x32x16_bf16 v[208:223], v[40:43], v[240:243], v[176:191]\n"
      "v_exp_f32 v229, v229\n"
      "v_exp_f32 v230, v230\n"
      "v_exp_f32 v231, v231\n"
      "v_exp_f32 v232, v232\n"
      "v_exp_f32 v233, v233\n"
      "v_exp_f32 v234, v234\n"
      "v_smfmac_f32_32x32x32_bf16 v[160:175], v[104:107], v[224:231], v28\n"
      "s_getpc_b64 vcc\n98:\ns_add_u32 vcc_lo, vcc_lo, 99f-98b\ns_addc_u32 vcc_hi, vcc_hi, 0\ns_load_dword %[dum], vcc, %[tc]\n"
      "v_exp_f32 v235, v235\n"
      "v_exp_f32 v236, v236\n"
      "v_exp_f32 v237, v237\n"
      "v_exp_f32 v238, v238\n"
      "v_exp_f32 v239, v239\n"
      "v_exp_f32 v208, v208\n"
      "v_exp_f32 v209, v209\n"
      "v_smfmac_f32_32x32x32_bf16 v[160:175], v[108:111], v[232:239], v28\n"
      "buffer_load_dwordx4 v[100:103], v16, %[rws], %[so] offen sc1\n"
      "s_mov_b64 exec, %[msk]\n"
      "buffer_load_dwordx4 v[104:107], v17, %[rws], %[so] offen offset:1024 sc1\n"
      "buffer_load_dwordx4 v[108:111], v17, %[rws], %[so] offen offset:1536 sc1\n"
      "s_mov_b64 exec, -1\n"
      "s_add_u32 %[so], %[so], 4096\n"
      "s_and_b32 %[so], %[so], 0x1ffff\n"
      "v_exp_f32 v210, v210\n"
      "v_exp_f32 v211, v211\n"
      "v_exp_f32 v212, v212\n"
      "v_mfma_f32_32x32x16_bf16 v[224:239], v[40:43], v[244:247], v[192:207]\n"
      "buffer_load_dwordx4 v[40:43], v16, %[rws], %[so] offen sc1\n"
      "v_exp_f32 v213, v213\n"
      "v_exp_f32 v214, v214\n"
      "v_exp_f32 v215, v215\n"
      "v_exp_f32 v216, v216\n"
      "v_exp_f32 v217, v217\n"
      "v_exp_f32 v218, v218\n"
      "v_smfmac_f32_32x32x32_bf16 v[144:159], v[44:47], v[208:215], v28\n"
      "s_buffer_load_dword %[dum], s[64:67], %[to2] offset:0\n"
      "v_exp_f32 v219, v219\n"
      "v_exp_f32 v220, v220\n"
      "v_exp_f32 v221, v221\n"
      "v_exp_f32 v222, v222\n"
      "v_exp_f32 v223, v223\n"
      "v_exp_f32 v224, v224\n"
      "v_exp_f32 v225, v225\n"
      "v_smfmac_f32_32x32x32_bf16 v[144:159], v[48:51], v[216:223], v28\n"
      "s_buffer_load_dword %[dum], s[64:67], %[to2] offset:1024\n"
      "v_exp_f32 v226, v226\n"
      "v_exp_f32 v227, v227\n"
      "v_exp_f32 v228, v228\n"
      "s_waitcnt vmcnt(14)\n"
      "v_mfma_f32_32x32x16_bf16 v[208:223], v[52:55], v[240:243], v[176:191]\n"
      "v_exp_f32 v229, v229\n"
      "v_exp_f32 v230, v230\n"
      "v_exp_f32 v231, v231\n"
      "v_exp_f32 v232, v232\n"
      "v_exp_f32 v233, v233\n"
      "v_exp_f32 v234, v234\n"
      "v_smfmac_f32_32x32x32_bf16 v[160:175], v[44:47], v[224:231], v28\n"
      "s_buffer_load_dword %[dum], s[64:67], %[to2] offset:2048\n"
      "v_exp_f32 v235, v235\n"
      "v_exp_f32 v236, v236\n"
      "v_exp_f32 v237, v237\n"
      "v_exp_f32 v238, v238\n"
      "v_exp_f32 v239, v239\n"
      "v_exp_f32 v208, v208\n"
      "v_exp_f32 v209, v209\n"
      "v_smfmac_f32_32x32x32_bf16 v[160:175], v[48:51], v[232:239], v28\n"
      "s_mov_b64 exec, %[msk]\n"
      "buffer_load_dwordx4 v[44:47], v17, %[rws], %[so] offen offset:1024 sc1\n"
      "buffer_load_dwordx4 v[48:51], v17, %[rws], %[so] offen offset:1536 sc1\n"
      "s_mov_b64 exec, -1\n"
      "s_add_u32 %[so], %[so], 4096\n"
      "s_and_b32 %[so], %[so], 0x1ffff\n"
      "v_exp_f32 v210, v210\n"
      "v_exp_f32 v211, v211\n"
      "v_exp_f32 v212, v212\n"
      "v_mfma_f32_32x32x16_bf16 v[224:239], v[52:55], v[244:247], v[192:207]\n"
      "buffer_load_dwordx4 v[52:55], v16, %[rws], %[so] offen sc1\n"
      "v_exp_f32 v213, v213\n"
      "v_exp_f32 v214, v214\n"
      "v_exp_f32 v215, v215\n"
      "v_exp_f32 v216, v216\n"
      "v_exp_f32 v217, v217\n"
      "v_exp_f32 v218, v218\n"
      "v_smfmac_f32_32x32x32_bf16 v[144:159], v[56:59], v[208:215], v28\n"
      "s_buffer_load_dword %[dum], s[64:67], %[to2] offset:3072\n"
      "v_exp_f32 v219, v219\n"
      "v_exp_f32 v220, v220\n"
      "v_exp_f32 v221, v221\n"
      "v_exp_f32 v222, v222\n"
      "v_exp_f32 v223, v223\n"
      "v_exp_f32 v224, v224\n"
      "v_exp_f32 v225, v225\n"
      "v_smfmac_f32_32x32x32_bf16 v[144:159], v[60:63], v[216:223], v28\n"
      "s_buffer_load_dword %[dum], s[64:67], %[to2] offset:4096\n"
      "v_exp_f32 v226, v226\n"
      "v_exp_f32 v227, v227\n"
      "v_exp_f32 v228, v228\n"
      "s_waitcnt vmcnt(14)\n"
      "v_mfma_f32_32x32x16_bf16 v[208:223], v[76:79], v[240:243], v[176:191]\n"
      "v_exp_f32 v229, v229\n"
      "v_exp_f32 v230, v230\n"
      "v_exp_f32 v231, v231\n"
      "v_exp_f32 v232, v232\n"
      "v_exp_f32 v233, v233\n"
      "v_exp_f32 v234, v234\n"
      "v_smfmac_f32_32x32x32_bf16 v[160:175], v[56:59], v[224:231], v28\n"
      "s_buffer_load_dword %[dum], s[64:67], %[to2] offset:5120\n"
      "v_exp_f32 v235, v235\n"
      "v_exp_f32 v236, v236\n"
      "v_exp_f32 v237, v237\n"
      "v_exp_f32 v238, v238\n"
      "v_exp_f32 v239, v239\n"
      "v_exp_f32 v208, v208\n"
      "v_exp_f32 v209, v209\n"
      "v_smfmac_f32_32x32x32_bf16 v[160:175], v[60:63], v[232:239], v28\n"
      "s_mov_b64 exec, %[msk]\n"
      "buffer_load_dwordx4 v[56:59], v17, %[rws], %[so] offen offset:1024 sc1\n"
      "buffer_load_dwordx4 v[60:63], v17, %[rws], %[so] offen offset:1536 sc1\n"
      "s_mov_b64 exec, -1\n"
      "s_add_u32 %[so], %[so], 4096\n"
      "s_and_b32 %[so], %[so], 0x1ffff\n"
      "v_exp_f32 v210, v210\n"
      "v_exp_f32 v211, v211\n"
      "v_exp_f32 v212, v212\n"
      "v_mfma_f32_32x32x16_bf16 v[224:239], v[76:79], v[244:247], v[192:207]\n"
      "buffer_load_dwordx4 v[76:79], v16, %[rws], %[so] offen sc1\n"
      "v_exp_f32 v213, v213\n"
      "v_exp_f32 v214, v214\n"
      "v_exp_f32 v215, v215\n"
      "v_exp_f32 v216, v216\n"
      "v_exp_f32 v217, v217\n"
      "v_exp_f32 v218, v218\n"
      "v_smfmac_f32_32x32x32_bf16 v[144:159], v[80:83], v[208:215], v28\n"
      "s_buffer_load_dword %[dum], s[64:67], %[to2] offset:6144\n"
      "v_exp_f32 v219, v219\n"
      "v_exp_f32 v220, v220\n"
      "v_exp_f32 v221, v221\n"
      "v_exp_f32 v222, v222\n"
      "v_exp_f32 v223, v223\n"
      "v_exp_f32 v224, v224\n"
      "v_exp_f32 v225, v225\n"
      "v_smfmac_f32_32x32x32_bf16 v[144:159], v[84:87], v[216:223], v28\n"
      "s_buffer_load_dword %[dum], s[64:67], %[to2] offset:7168\n"
      "v_exp_f32 v226, v226\n"
      "v_exp_f32 v227, v227\n"
      "v_exp_f32 v228, v228\n"
      "s_waitcnt vmcnt(14)\n"
      "v_mfma_f32_32x32x16_bf16 v[208:223], v[64:67], v[240:243], v[176:191]\n"
      "v_exp_f32 v229, v229\n"
      "v_exp_f32 v230, v230\n"
      "v_exp_f32 v231, v231\n"
      "v_exp_f32 v232, v232\n"
      "v_exp_f32 v233, v233\n"
      "v_exp_f32 v234, v234\n"
      "v_smfmac_f32_32x32x32_bf16 v[160:175], v[80:83], v[224:231], v28\n"
      "s_buffer_load_dword %[dum], s[64:67], %[to2] offset:8192\n"
      "v_exp_f32 v235, v235\n"
      "v_exp_f32 v236, v236\n"
      "v_exp_f32 v237, v237\n"
      "v_exp_f32 v238, v238\n"
      "v_exp_f32 v239, v239\n"
      "v_exp_f32 v208, v208\n"
      "v_exp_f32 v209, v209\n"
      "v_smfmac_f32_32x32x32_bf16 v[160:175], v[84:87], v[232:239], v28\n"
      "s_mov_b64 exec, %[msk]\n"
      "buffer_load_dwordx4 v[80:83], v17, %[rws], %[so] offen offset:1024 sc1\n"
      "buffer_load_dwordx4 v[84:87], v17, %[rws], %[so] offen offset:1536 sc1\n"
      "s_mov_b64 exec, -1\n"
      "s_add_u32 %[so], %[so], 4096\n"
      "s_and_b32 %[so], %[so], 0x1ffff\n"
      "v_exp_f32 v210, v210\n"
      "v_exp_f32 v211, v211\n"
      "v_exp_f32 v212, v212\n"
      "v_mfma_f32_32x32x16_bf16 v[224:239], v[64:67], v[244:247], v[192:207]\n"
      "buffer_load_dwordx4 v[64:67], v16, %[rws], %[so] offen sc1\n"
      "v_exp_f32 v213, v213\n"
      "v_exp_f32 v214, v214\n"
      "v_exp_f32 v215, v215\n"
      "v_exp_f32 v216, v216\n"
      "v_exp_f32 v217, v217\n"
      "v_exp_f32 v218, v218\n"
      "v_smfmac_f32_32x32x32_bf16 v[144:159], v[68:71], v[208:215], v28\n"
      "s_buffer_load_dword %[dum], s[64:67], %[to2] offset:9216\n"
      "v_exp_f32 v219, v219\n"
      "v_exp_f32 v220, v220\n"
      "v_exp_f32 v221, v221\n"
      "v_exp_f32 v222, v222\n"
      "v_exp_f32 v223, v223\n"
      "v_exp_f32 v224, v224\n"
      "v_exp_f32 v225, v225\n"
      "v_smfmac_f32_32x32x32_bf16 v[144:159], v[72:75], v[216:223], v28\n"
      "s_buffer_load_dword %[dum], s[64:67], %[to2] offset:10240\n"
      "v_exp_f32 v226, v226\n"
      "v_exp_f32 v227, v227\n"
      "v_exp_f32 v228, v228\n"
      "s_waitcnt vmcnt(14)\n"
      "v_mfma_f32_32x32x16_bf16 v[208:223], v[88:91], v[240:243], v[176:191]\n"
      "v_exp_f32 v229, v229\n"
      "v_exp_f32 v230, v230\n"
      "v_exp_f32 v231, v231\n"
      "v_exp_f32 v232, v232\n"
      "v_exp_f32 v233, v233\n"
      "v_exp_f32 v234, v234\n"
      "v_smfmac_f32_32x32x32_bf16 v[160:175], v[68:71], v[224:231], v28\n"
      "s_buffer_load_dword %[dum], s[64:67], %[to2] offset:11264\n"
      "v_exp_f32 v235, v235\n"
      "v_exp_f32 v236, v236\n"
      "v_exp_f32 v237, v237\n"
      "v_exp_f32 v238, v238\n"
      "v_exp_f32 v239, v239\n"
      "v_exp_f32 v208, v208\n"
      "v_exp_f32 v209, v209\n"
      "v_smfmac_f32_32x32x32_bf16 v[160:175], v[72:75], v[232:239], v28\n"
      "s_mov_b64 exec, %[msk]\n"
      "buffer_load_dwordx4 v[68:71], v17, %[rws], %[so] offen offset:1024 sc1\n"
      "buffer_load_dwordx4 v[72:75], v17, %[rws], %[so] offen offset:1536 sc1\n"
      "s_mov_b64 exec, -1\n"
      "s_add_u32 %[so], %[so], 4096\n"
      "s_and_b32 %[so], %[so], 0x1ffff\n"
      "v_exp_f32 v210, v210\n"
      "v_exp_f32 v211, v211\n"
      "v_exp_f32 v212, v212\n"
      "v_mfma_f32_32x32x16_bf16 v[224:239], v[88:91], v[244:247], v[192:207]\n"
      "buffer_load_dwordx4 v[88:91], v16, %[rws], %[so] offen sc1\n"
      "v_exp_f32 v213, v213\n"
      "v_exp_f32 v214, v214\n"
      "v_exp_f32 v215, v215\n"
      "v_exp_f32 v216, v216\n"
      "v_exp_f32 v217, v217\n"
      "v_exp_f32 v218, v218\n"
      "v_smfmac_f32_32x32x32_bf16 v[144:159], v[92:95], v[208:215], v28\n"
      "s_buffer_load_dword %[dum], s[64:67], %[to2] offset:12288\n"
      "v_exp_f32 v219, v219\n"
      "v_exp_f32 v220, v220\n"
      "v_exp_f32 v221, v221\n"
      "v_exp_f32 v222, v222\n"
      "v_exp_f32 v223, v223\n"
      "v_exp_f32 v224, v224\n"
      "v_exp_f32 v225, v225\n"
      "v_smfmac_f32_32x32x32_bf16 v[144:159], v[96:99], v[216:223], v28\n"
      "s_buffer_load_dword %[dum], s[64:67], %[to2] offset:13312\n"
      "buffer_load_dwordx4 v[12:15], v22, %[rwr], 0 offen\n"
      "v_exp_f32 v226, v226\n"
      "v_exp_f32 v227, v227\n"
      "v_exp_f32 v228, v228\n"
      "s_waitcnt vmcnt(14)\n"
      "v_mfma_f32_32x32x16_bf16 v[208:223], v[100:103], v[240:243], v[176:191]\n"
      "v_exp_f32 v229, v229\n"
      "v_exp_f32 v230, v230\n"
      "v_exp_f32 v231, v231\n"
      "v_exp_f32 v232, v232\n"
      "v_exp_f32 v233, v233\n"
      "v_exp_f32 v234, v234\n"
      "v_smfmac_f32_32x32x32_bf16 v[160:175], v[92:95], v[224:231], v28\n"
      "s_buffer_load_dword %[dum], s[56:59], %[to1] offset:0\n"
      "v_exp_f32 v235, v235\n"
      "v_exp_f32 v236, v236\n"
      "v_exp_f32 v237, v237\n"
      "v_exp_f32 v238, v238\n"
      "v_exp_f32 v239, v239\n"
      "v_exp_f32 v208, v208\n"
      "v_exp_f32 v209, v209\n"
      "v_smfmac_f32_32x32x32_bf16 v[160:175], v[96:99], v[232:239], v28\n"
      "s_mov_b64 exec, %[msk]\n"
      "buffer_load_dwordx4 v[92:95], v17, %[rws], %[so] offen offset:1024 sc1\n"
      "buffer_load_dwordx4 v[96:99], v17, %[rws], %[so] offen offset:1536 sc1\n"
      "s_mov_b64 exec, -1\n"
      "s_add_u32 %[so], %[so], 4096\n"
      "s_and_b32 %[so], %[so], 0x1ffff\n"
      "v_exp_f32 v210, v210\n"
      "v_exp_f32 v211, v211\n"
      "v_exp_f32 v212, v212\n"
      "v_mfma_f32_32x32x16_bf16 v[224:239], v[100:103], v[244:247], v[192:207]\n"
      "buffer_load_dwordx4 v[100:103], v16, %[rws], %[so] offen sc1\n"
      "v_exp_f32 v213, v213\n"
      "v_exp_f32 v214, v214\n"
      "v_exp_f32 v215, v215\n"
      "v_exp_f32 v216, v216\n"
      "v_exp_f32 v217, v217\n"
      "v_exp_f32 v218, v218\n"
      "v_smfmac_f32_32x32x32_bf16 v[144:159], v[104:107], v[208:215], v28\n"
      "s_buffer_load_dword %[dum], s[56:59], %[to1] offset:3584\n"
      "v_exp_f32 v219, v219\n"
      "v_exp_f32 v220, v220\n"
      "v_exp_f32 v221, v221\n"
      "v_exp_f32 v222, v222\n"
      "v_exp_f32 v223, v223\n"
      "v_exp_f32 v224, v224\n"
      "v_exp_f32 v225, v225\n"
      "v_smfmac_f32_32x32x32_bf16 v[144:159], v[108:111], v[216:223], v28\n"
      "s_buffer_load_dword %[dum], s[56:59], %[to1] offset:7168\n"
      "v_exp_f32 v226, v226\n"
      "v_exp_f32 v227, v227\n"
      "v_exp_f32 v228, v228\n"
      "s_waitcnt vmcnt(14)\n"
      "v_mfma_f32_32x32x16_bf16 v[208:223], v[40:43], v[240:243], v[176:191]\n"
      "v_exp_f32 v229, v229\n"
      "v_exp_f32 v230, v230\n"
      "v_exp_f32 v231, v231\n"
      "v_exp_f32 v232, v232\n"
      "v_exp_f32 v233, v233\n"
      "v_exp_f32 v234, v234\n"
      "v_smfmac_f32_32x32x32_bf16 v[160:175], v[104:107], v[224:231], v28\n"
      "s_buffer_load_dword %[dum], s[56:59], %[to1] offset:10752\n"
      "v_exp_f32 v235, v235\n"
      "v_exp_f32 v236, v236\n"
      "v_exp_f32 v237, v237\n"
      "v_exp_f32 v238, v238\n"
      "v_exp_f32 v239, v239\n"
      "v_exp_f32 v208, v208\n"
      "v_exp_f32 v209, v209\n"
      "v_smfmac_f32_32x32x32_bf16 v[160:175], v[108:111], v[232:239], v28\n"
      "s_mov_b64 exec, %[msk]\n"
      "buffer_load_dwordx4 v[104:107], v17, %[rws], %[so] offen offset:1024 sc1\n"
      "buffer_load_dwordx4 v[108:111], v17, %[rws], %[so] offen offset:1536 sc1\n"
      "s_mov_b64 exec, -1\n"
      "s_add_u32 %[so], %[so], 4096\n"
      "s_and_b32 %[so], %[so], 0x1ffff\n"
      "v_exp_f32 v210, v210\n"
      "v_exp_f32 v211, v211\n"
      "v_exp_f32 v212, v212\n"
      "v_mfma_f32_32x32x16_bf16 v[224:239], v[40:43], v[244:247], v[192:207]\n"
      "buffer_load_dwordx4 v[40:43], v16, %[rws], %[so] offen sc1\n"
      "v_exp_f32 v213, v213\n"
      "v_exp_f32 v214, v214\n"
      "v_exp_f32 v215, v215\n"
      "v_exp_f32 v216, v216\n"
      "v_exp_f32 v217, v217\n"
      "v_exp_f32 v218, v218\n"
      "v_smfmac_f32_32x32x32_bf16 v[144:159], v[44:47], v[208:215], v28\n"
      "s_buffer_load_dword %[dum], s[56:59], %[to1] offset:14336\n"
      "s_lshl_b32 %[tmp], %[wi], 11\n"
      "s_add_u32 m0, %[tmp], %[wm]\n"
      "s_lshl_b32 %[tmp], %[wi], 13\n"
      "buffer_load_dwordx4 v18, s[52:55], %[tmp] offen lds\n"
      "v_exp_f32 v219, v219\n"
      "v_exp_f32 v220, v220\n"
      "v_exp_f32 v221, v221\n"
      "v_exp_f32 v222, v222\n"
      "v_exp_f32 v223, v223\n"
      "v_exp_f32 v224, v224\n"
      "v_exp_f32 v225, v225\n"
      "v_smfmac_f32_32x32x32_bf16 v[144:159], v[48:51], v[216:223], v28\n"
      "v_mov_b64 v[32:33], %[tag]\n"
      "s_mov_b64 exec, 1\n"
      "buffer_store_dwordx2 v[32:33], off, %[rws], %[srf] sc1\n"
      "s_mov_b64 exec, -1\n"
      "v_exp_f32 v226, v226\n"
      "v_exp_f32 v227, v227\n"
      "v_exp_f32 v228, v228\n"
      "s_waitcnt vmcnt(16)\n"
      "v_mfma_f32_32x32x16_bf16 v[208:223], v[52:55], v[240:243], v[176:191]\n"
      "v_exp_f32 v229, v229\n"
      "v_exp_f32 v230, v230\n"
      "v_exp_f32 v231, v231\n"
      "v_exp_f32 v232, v232\n"
      "v_exp_f32 v233, v233\n"
      "v_exp_f32 v234, v234\n"
      "v_smfmac_f32_32x32x32_bf16 v[160:175], v[44:47], v[224:231], v28\n"
      "s_buffer_load_dword %[dum], s[56:59], %[to1] offset:17920\n"
      "v_exp_f32 v235, v235\n"
      "v_exp_f32 v236, v236\n"
      "v_exp_f32 v237, v237\n"
      "v_exp_f32 v238, v238\n"
      "v_exp_f32 v239, v239\n"
      "v_exp_f32 v208, v208\n"
      "v_exp_f32 v209, v209\n"
      "v_smfmac_f32_32x32x32_bf16 v[160:175], v[48:51], v[232:239], v28\n"
      "s_mov_b64 exec, %[msk]\n"
      "buffer_load_dwordx4 v[44:47], v17, %[rws], %[so] offen offset:1024 sc1\n"
      "buffer_load_dwordx4 v[48:51], v17, %[rws], %[so] offen offset:1536 sc1\n"
      "s_mov_b64 exec, -1\n"
      "s_add_u32 %[so], %[so], 4096\n"
      "s_and_b32 %[so], %[so], 0x1ffff\n"
      "v_exp_f32 v210, v210\n"
      "v_exp_f32 v211, v211\n"
      "v_exp_f32 v212, v212\n"
      "v_mfma_f32_32x32x16_bf16 v[224:239], v[52:55], v[244:247], v[192:207]\n"
      "buffer_load_dwordx4 v[52:55], v16, %[rws], %[so] offen sc1\n"
      "v_exp_f32 v213, v213\n"
      "v_exp_f32 v214, v214\n"
      "v_exp_f32 v215, v215\n"
      "v_exp_f32 v216, v216\n"
      "v_exp_f32 v217, v217\n"
      "v_exp_f32 v218, v218\n"
      "v_smfmac_f32_32x32x32_bf16 v[144:159], v[56:59], v[208:215], v28\n"
      "s_buffer_load_dword %[dum], s[56:59], %[to1] offset:21504\n"
      "s_lshl_b32 %[tmp], %[wi], 11\n"
      "s_add_u32 m0, %[tmp], %[wm]\n"
      "s_add_u32 m0, m0, 1024\n"
      "s_lshl_b32 %[tmp], %[wi], 13\n"
      "buffer_load_dwordx4 v19, s[52:55], %[tmp] offen lds\n"
      "s_add_u32 %[wi], %[wi], 1\n"
      "s_and_b32 %[wi], %[wi], 7\n"
      "v_exp_f32 v219, v219\n"
      "v_exp_f32 v220, v220\n"
      "v_exp_f32 v221, v221\n"
      "v_exp_f32 v222, v222\n"
      "v_exp_f32 v223, v223\n"
      "v_exp_f32 v224, v224\n"
      "v_exp_f32 v225, v225\n"
      "v_smfmac_f32_32x32x32_bf16 v[144:159], v[60:63], v[216:223], v28\n"
      "v_exp_f32 v226, v226\n"
      "v_exp_f32 v227, v227\n"
      "v_exp_f32 v228, v228\n"
      "s_waitcnt vmcnt(17)\n"
      "v_mfma_f32_32x32x16_bf16 v[208:223], v[76:79], v[240:243], v[176:191]\n"
      "v_exp_f32 v229, v229\n"
      "v_exp_f32 v230, v230\n"
      "v_exp_f32 v231, v231\n"
      "v_exp_f32 v232, v232\n"
      "v_exp_f32 v233, v233\n"
      "v_exp_f32 v234, v234\n"
      "v_smfmac_f32_32x32x32_bf16 v[160:175], v[56:59], v[224:231], v28\n"
      "s_buffer_load_dword %[dum], s[56:59], %[to1] offset:25088\n"
      "v_exp_f32 v235, v235\n"
      "v_exp_f32 v236, v236\n"
      "v_exp_f32 v237, v237\n"
      "v_exp_f32 v238, v238\n"
      "v_exp_f32 v239, v239\n"
      "v_exp_f32 v208, v208\n"
      "v_exp_f32 v209, v209\n"
      "v_smfmac_f32_32x32x32_bf16 v[160:175], v[60:63], v[232:239], v28\n"
      "s_mov_b64 exec, %[msk]\n"
      "buffer_load_dwordx4 v[56:59], v17, %[rws], %[so] offen offset:1024 sc1\n"
      "buffer_load_dwordx4 v[60:63], v17, %[rws], %[so] offen offset:1536 sc1\n"
      "s_mov_b64 exec, -1\n"
      "s_add_u32 %[so], %[so], 4096\n"
      "s_and_b32 %[so], %[so], 0x1ffff\n"
      "v_exp_f32 v210, v210\n"
      "v_exp_f32 v211, v211\n"
      "v_exp_f32 v212, v212\n"
      "v_mfma_f32_32x32x16_bf16 v[224:239], v[76:79], v[244:247], v[192:207]\n"
      "buffer_load_dwordx4 v[76:79], v16, %[rws], %[so] offen sc1\n"
      "v_exp_f32 v213, v213\n"
      "v_exp_f32 v214, v214\n"
      "v_exp_f32 v215, v215\n"
      "v_exp_f32 v216, v216\n"
      "v_exp_f32 v217, v217\n"
      "v_exp_f32 v218, v218\n"
      "v_smfmac_f32_32x32x32_bf16 v[144:159], v[80:83], v[208:215], v28\n"
      "s_buffer_load_dword %[dum], s[56:59], %[to1] offset:28672\n"
      "s_lshl_b32 %[tmp], %[wi], 11\n"
      "s_add_u32 m0, %[tmp], %[wm]\n"
      "s_lshl_b32 %[tmp], %[wi], 13\n"
      "buffer_load_dwordx4 v18, s[52:55], %[tmp] offen lds\n"
      "v_exp_f32 v219, v219\n"
      "v_exp_f32 v220, v220\n"
      "v_exp_f32 v221, v221\n"
      "v_exp_f32 v222, v222\n"
      "v_exp_f32 v223, v223\n"
      "v_exp_f32 v224, v224\n"
      "v_exp_f32 v225, v225\n"
      "v_smfmac_f32_32x32x32_bf16 v[144:159], v[84:87], v[216:223], v28\n"
      "buffer_load_dwordx2 v[34:35], v21, %[rx], 0 offen\n"
      "v_exp_f32 v226, v226\n"
      "v_exp_f32 v227, v227\n"
      "v_exp_f32 v228, v228\n"
      "s_waitcnt vmcnt(19)\n"
      "v_mfma_f32_32x32x16_bf16 v[208:223], v[64:67], v[240:243], v[176:191]\n"
      "v_exp_f32 v229, v229\n"
      "v_exp_f32 v230, v230\n"
      "v_exp_f32 v231, v231\n"
      "v_exp_f32 v232, v232\n"
      "v_exp_f32 v233, v233\n"
      "v_exp_f32 v234, v234\n"
      "v_smfmac_f32_32x32x32_bf16 v[160:175], v[80:83], v[224:231], v28\n"
      "s_buffer_load_dword %[dum], s[56:59], %[to1] offset:32256\n"
      "v_exp_f32 v235, v235\n"
      "v_exp_f32 v236, v236\n"
      "v_exp_f32 v237, v237\n"
      "v_exp_f32 v238, v238\n"
      "v_exp_f32 v239, v239\n"
      "v_exp_f32 v208, v208\n"
      "v_exp_f32 v209, v209\n"
      "v_smfmac_f32_32x32x32_bf16 v[160:175], v[84:87], v[232:239], v28\n"
      "s_mov_b64 exec, %[msk]\n"
      "buffer_load_dwordx4 v[80:83], v17, %[rws], %[so] offen offset:1024 sc1\n"
      "buffer_load_dwordx4 v[84:87], v17, %[rws], %[so] offen offset:1536 sc1\n"
      "s_mov_b64 exec, -1\n"
      "s_add_u32 %[so], %[so], 4096\n"
      "s_and_b32 %[so], %[so], 0x1ffff\n"
      "v_exp_f32 v210, v210\n"
      "v_exp_f32 v211, v211\n"
      "v_exp_f32 v212, v212\n"
      "v_mfma_f32_32x32x16_bf16 v[224:239], v[64:67], v[244:247], v[192:207]\n"
      "buffer_load_dwordx4 v[64:67], v16, %[rws], %[so] offen sc1\n"
      "v_exp_f32 v213, v213\n"
      "v_exp_f32 v214, v214\n"
      "v_exp_f32 v215, v215\n"
      "v_exp_f32 v216, v216\n"
      "v_exp_f32 v217, v217\n"
      "v_exp_f32 v218, v218\n"
      "v_smfmac_f32_32x32x32_bf16 v[144:159], v[68:71], v[208:215], v28\n"
      "s_buffer_load_dword %[dum], s[56:59], %[to1] offset:35840\n"
      "s_lshl_b32 %[tmp], %[wi], 11\n"
      "s_add_u32 m0, %[tmp], %[wm]\n"
      "s_add_u32 m0, m0, 1024\n"
      "s_lshl_b32 %[tmp], %[wi], 13\n"
      "buffer_load_dwordx4 v19, s[52:55], %[tmp] offen lds\n"
      "s_add_u32 %[wi], %[wi], 1\n"
      "s_and_b32 %[wi], %[wi], 7\n"
      "v_exp_f32 v219, v219\n"
      "v_exp_f32 v220, v220\n"
      "v_exp_f32 v221, v221\n"
      "v_exp_f32 v222, v222\n"
      "v_exp_f32 v223, v223\n"
      "v_exp_f32 v224, v224\n"
      "v_exp_f32 v225, v225\n"
      "v_smfmac_f32_32x32x32_bf16 v[144:159], v[72:75], v[216:223], v28\n"
      "s_movk_i32 %[tmp], 16384\n"
      "buffer_load_dwordx2 v[36:37], v21, %[rx], %[tmp] offen\n"
      "v_exp_f32 v226, v226\n"
      "v_exp_f32 v227, v227\n"
      "v_exp_f32 v228, v228\n"
      "s_waitcnt vmcnt(20)\n"
      "v_mfma_f32_32x32x16_bf16 v[208:223], v[88:91], v[240:243], v[176:191]\n"
      "v_exp_f32 v229, v229\n"
      "v_exp_f32 v230, v230\n"
      "v_exp_f32 v231, v231\n"
      "v_exp_f32 v232, v232\n"
      "v_exp_f32 v233, v233\n"
      "v_exp_f32 v234, v234\n"
      "v_smfmac_f32_32x32x32_bf16 v[160:175], v[68:71], v[224:231], v28\n"
      "s_buffer_load_dword %[dum], s[56:59], %[to1] offset:39424\n"
      "v_exp_f32 v235, v235\n"
      "v_exp_f32 v236, v236\n"
      "v_exp_f32 v237, v237\n"
      "v_exp_f32 v238, v238\n"
      "v_exp_f32 v239, v239\n"
      "v_exp_f32 v208, v208\n"
      "v_exp_f32 v209, v209\n"
      "v_smfmac_f32_32x32x32_bf16 v[160:175], v[72:75], v[232:239], v28\n"
      "s_mov_b64 exec, %[msk]\n"
      "buffer_load_dwordx4 v[68:71], v17, %[rws], %[so] offen offset:1024 sc1\n"
      "buffer_load_dwordx4 v[72:75], v17, %[rws], %[so] offen offset:1536 sc1\n"
      "s_mov_b64 exec, -1\n"
      "s_add_u32 %[so], %[so], 4096\n"
      "s_and_b32 %[so], %[so], 0x1ffff\n"
      "v_exp_f32 v210, v210\n"
      "v_exp_f32 v211, v211\n"
      "v_exp_f32 v212, v212\n"
      "v_mfma_f32_32x32x16_bf16 v[224:239], v[88:91], v[244:247], v[192:207]\n"
      "buffer_load_dwordx4 v[88:91], v16, %[rws], %[so] offen sc1\n"
      "v_exp_f32 v213, v213\n"
      "v_exp_f32 v214, v214\n"
      "v_exp_f32 v215, v215\n"
      "v_exp_f32 v216, v216\n"
      "v_exp_f32 v217, v217\n"
      "v_exp_f32 v218, v218\n"
      "v_smfmac_f32_32x32x32_bf16 v[144:159], v[92:95], v[208:215], v28\n"
      "s_buffer_load_dword %[dum], s[56:59], %[to1] offset:43008\n"
      "s_lshl_b32 %[tmp], %[wi], 11\n"
      "s_add_u32 m0, %[tmp], %[wm]\n"
      "s_lshl_b32 %[tmp], %[wi], 13\n"
      "buffer_load_dwordx4 v18, s[52:55], %[tmp] offen lds\n"
      "v_exp_f32 v219, v219\n"
      "v_exp_f32 v220, v220\n"
      "v_exp_f32 v221, v221\n"
      "v_exp_f32 v222, v222\n"
      "v_exp_f32 v223, v223\n"
      "v_exp_f32 v224, v224\n"
      "v_exp_f32 v225, v225\n"
      "v_smfmac_f32_32x32x32_bf16 v[144:159], v[96:99], v[216:223], v28\n"
      "v_exp_f32 v226, v226\n"
      "v_exp_f32 v227, v227\n"
      "v_exp_f32 v228, v228\n"
      "s_waitcnt vmcnt(21)\n"
      "v_mfma_f32_32x32x16_bf16 v[208:223], v[100:103], v[240:243], v[176:191]\n"
      "v_exp_f32 v229, v229\n"
      "v_exp_f32 v230, v230\n"
      "v_exp_f32 v231, v231\n"
      "v_exp_f32 v232, v232\n"
      "v_exp_f32 v233, v233\n"
      "v_exp_f32 v234, v234\n"
      "v_smfmac_f32_32x32x32_bf16 v[160:175], v[92:95], v[224:231], v28\n"
      "s_buffer_load_dword %[dum], s[56:59], %[to1] offset:46592\n"
      "v_exp_f32 v235, v235\n"
      "v_exp_f32 v236, v236\n"
      "v_exp_f32 v237, v237\n"
      "v_exp_f32 v238, v238\n"
      "v_exp_f32 v239, v239\n"
      "v_exp_f32 v208, v208\n"
      "v_exp_f32 v209, v209\n"
      "v_smfmac_f32_32x32x32_bf16 v[160:175], v[96:99], v[232:239], v28\n"
      "s_mov_b64 exec, %[msk]\n"
      "buffer_load_dwordx4 v[92:95], v17, %[rws], %[so] offen offset:1024 sc1\n"
      "buffer_load_dwordx4 v[96:99], v17, %[rws], %[so] offen offset:1536 sc1\n"
      "s_mov_b64 exec, -1\n"
      "s_add_u32 %[so], %[so], 4096\n"
      "s_and_b32 %[so], %[so], 0x1ffff\n"
      "v_exp_f32 v210, v210\n"
      "v_exp_f32 v211, v211\n"
      "v_exp_f32 v212, v212\n"
      "v_mfma_f32_32x32x16_bf16 v[224:239], v[100:103], v[244:247], v[192:207]\n"
      "buffer_load_dwordx4 v[100:103], v16, %[rws], %[so] offen sc1\n"
      "v_exp_f32 v213, v213\n"
      "v_exp_f32 v214, v214\n"
      "v_exp_f32 v215, v215\n"
      "v_exp_f32 v216, v216\n"
      "v_exp_f32 v217, v217\n"
      "v_exp_f32 v218, v218\n"
      "v_smfmac_f32_32x32x32_bf16 v[144:159], v[104:107], v[208:215], v28\n"
      "s_buffer_load_dword %[dum], s[56:59], %[to1] offset:50176\n"
      "s_lshl_b32 %[tmp], %[wi], 11\n"
      "s_add_u32 m0, %[tmp], %[wm]\n"
      "s_add_u32 m0, m0, 1024\n"
      "s_lshl_b32 %[tmp], %[wi], 13\n"
      "buffer_load_dwordx4 v19, s[52:55], %[tmp] offen lds\n"
      "s_add_u32 %[wi], %[wi], 1\n"
      "s_and_b32 %[wi], %[wi], 7\n"
      "v_exp_f32 v219, v219\n"
      "v_exp_f32 v220, v220\n"
      "v_exp_f32 v221, v221\n"
      "v_exp_f32 v222, v222\n"
      "v_exp_f32 v223, v223\n"
      "v_exp_f32 v224, v224\n"
      "v_exp_f32 v225, v225\n"
      "v_smfmac_f32_32x32x32_bf16 v[144:159], v[108:111], v[216:223], v28\n"
      "v_exp_f32 v226, v226\n"
      "v_exp_f32 v227, v227\n"
      "v_exp_f32 v228, v228\n"
      "s_waitcnt vmcnt(20)\n"
      "v_mfma_f32_32x32x16_bf16 v[208:223], v[40:43], v[240:243], v[176:191]\n"
      "v_exp_f32 v229, v229\n"
      "v_exp_f32 v230, v230\n"
      "v_exp_f32 v231, v231\n"
      "v_exp_f32 v232, v232\n"
      "v_exp_f32 v233, v233\n"
      "v_exp_f32 v234, v234\n"
      "v_smfmac_f32_32x32x32_bf16 v[160:175], v[104:107], v[224:231], v28\n"
      "s_buffer_load_dword %[dum], s[56:59], %[to1] offset:53760\n"
      "v_exp_f32 v235, v235\n"
      "v_exp_f32 v236, v236\n"
      "v_exp_f32 v237, v237\n"
      "v_exp_f32 v238, v238\n"
      "v_exp_f32 v239, v239\n"
      "v_exp_f32 v208, v208\n"
      "v_exp_f32 v209, v209\n"
      "v_smfmac_f32_32x32x32_bf16 v[160:175], v[108:111], v[232:239], v28\n"
      "s_mov_b64 exec, %[msk]\n"
      "buffer_load_dwordx4 v[104:107], v17, %[rws], %[so] offen offset:1024 sc1\n"
      "buffer_load_dwordx4 v[108:111], v17, %[rws], %[so] offen offset:1536 sc1\n"
      "s_mov_b64 exec, -1\n"
      "s_add_u32 %[so], %[so], 4096\n"
      "s_and_b32 %[so], %[so], 0x1ffff\n"
      "v_exp_f32 v210, v210\n"
      "v_exp_f32 v211, v211\n"
      "v_exp_f32 v212, v212\n"
      "v_mfma_f32_32x32x16_bf16 v[224:239], v[40:43], v[244:247], v[192:207]\n"
      "buffer_load_dwordx4 v[40:43], v16, %[rws], %[so] offen sc1\n"
      "v_exp_f32 v213, v213\n"
      "v_exp_f32 v214, v214\n"
      "v_exp_f32 v215, v215\n"
      "v_exp_f32 v216, v216\n"
      "v_exp_f32 v217, v217\n"
      "v_exp_f32 v218, v218\n"
      "v_smfmac_f32_32x32x32_bf16 v[144:159], v[44:47], v[208:215], v28\n"
      "s_buffer_load_dword %[dum], s[60:63], %[to1] offset:0\n"
      "s_lshl_b32 %[tmp], %[wi], 11\n"
      "s_add_u32 m0, %[tmp], %[wm]\n"
      "s_lshl_b32 %[tmp], %[wi], 13\n"
      "buffer_load_dwordx4 v18, s[52:55], %[tmp] offen lds\n"
      "v_exp_f32 v219, v219\n"
      "v_exp_f32 v220, v220\n"
      "v_exp_f32 v221, v221\n"
      "v_exp_f32 v222, v222\n"
      "v_exp_f32 v223, v223\n"
      "v_exp_f32 v224, v224\n"
      "v_exp_f32 v225, v225\n"
      "v_smfmac_f32_32x32x32_bf16 v[144:159], v[48:51], v[216:223], v28\n"
      "v_exp_f32 v226, v226\n"
      "v_exp_f32 v227, v227\n"
      "v_exp_f32 v228, v228\n"
      "s_waitcnt vmcnt(20)\n"
      "v_mfma_f32_32x32x16_bf16 v[208:223], v[52:55], v[240:243], v[176:191]\n"
      "v_exp_f32 v229, v229\n"
      "v_exp_f32 v230, v230\n"
      "v_exp_f32 v231, v231\n"
      "v_exp_f32 v232, v232\n"
      "v_exp_f32 v233, v233\n"
      "v_exp_f32 v234, v234\n"
      "v_smfmac_f32_32x32x32_bf16 v[160:175], v[44:47], v[224:231], v28\n"
      "s_buffer_load_dword %[dum], s[60:63], %[to1] offset:3584\n"
      "v_exp_f32 v235, v235\n"
      "v_exp_f32 v236, v236\n"
      "v_exp_f32 v237, v237\n"
      "v_exp_f32 v238, v238\n"
      "v_exp_f32 v239, v239\n"
      "v_exp_f32 v208, v208\n"
      "v_exp_f32 v209, v209\n"
      "v_smfmac_f32_32x32x32_bf16 v[160:175], v[48:51], v[232:239], v28\n"
      "s_mov_b64 exec, %[msk]\n"
      "buffer_load_dwordx4 v[44:47], v17, %[rws], %[so] offen offset:1024 sc1\n"
      "buffer_load_dwordx4 v[48:51], v17, %[rws], %[so] offen offset:1536 sc1\n"
      "s_mov_b64 exec, -1\n"
      "s_add_u32 %[so], %[so], 4096\n"
      "s_and_b32 %[so], %[so], 0x1ffff\n"
      "s_setprio 2\n"
      "v_exp_f32 v210, v210\n"
      "v_exp_f32 v211, v211\n"
      "v_exp_f32 v212, v212\n"
      "v_mfma_f32_32x32x16_bf16 v[224:239], v[52:55], v[244:247], v[192:207]\n"
      "buffer_load_dwordx4 v[52:55], v16, %[rws], %[so] offen sc1\n"
      "v_exp_f32 v213, v213\n"
      "v_exp_f32 v214, v214\n"
      "v_exp_f32 v215, v215\n"
      "v_exp_f32 v216, v216\n"
      "v_exp_f32 v217, v217\n"
      "v_exp_f32 v218, v218\n"
      "v_smfmac_f32_32x32x32_bf16 v[144:159], v[56:59], v[208:215], v28\n"
      "s_buffer_load_dword %[dum], s[60:63], %[to1] offset:7168\n"
      "s_lshl_b32 %[tmp], %[wi], 11\n"
      "s_add_u32 m0, %[tmp], %[wm]\n"
      "s_add_u32 m0, m0, 1024\n"
      "s_lshl_b32 %[tmp], %[wi], 13\n"
      "buffer_load_dwordx4 v19, s[52:55], %[tmp] offen lds\n"
      "s_add_u32 %[wi], %[wi], 1\n"
      "s_and_b32 %[wi], %[wi], 7\n"
      "v_exp_f32 v219, v219\n"
      "v_exp_f32 v220, v220\n"
      "v_exp_f32 v221, v221\n"
      "v_exp_f32 v222, v222\n"
      "v_exp_f32 v223, v223\n"
      "v_exp_f32 v224, v224\n"
      "v_exp_f32 v225, v225\n"
      "v_smfmac_f32_32x32x32_bf16 v[144:159], v[60:63], v[216:223], v28\n"
      "v_exp_f32 v226, v226\n"
      "v_exp_f32 v227, v227\n"
      "v_exp_f32 v228, v228\n"
      "s_waitcnt vmcnt(19)\n"
      "v_mfma_f32_32x32x16_bf16 v[208:223], v[76:79], v[240:243], v[176:191]\n"
      "v_exp_f32 v229, v229\n"
      "v_exp_f32 v230, v230\n"
      "v_exp_f32 v231, v231\n"
      "v_exp_f32 v232, v232\n"
      "v_exp_f32 v233, v233\n"
      "v_exp_f32 v234, v234\n"
      "v_smfmac_f32_32x32x32_bf16 v[160:175], v[56:59], v[224:231], v28\n"
      "s_buffer_load_dword %[dum], s[60:63], %[to1] offset:10752\n"
      "v_exp_f32 v235, v235\n"
      "v_exp_f32 v236, v236\n"
      "v_exp_f32 v237, v237\n"
      "v_exp_f32 v238, v238\n"
      "v_exp_f32 v239, v239\n"
      "v_exp_f32 v208, v208\n"
      "v_exp_f32 v209, v209\n"
      "v_smfmac_f32_32x32x32_bf16 v[160:175], v[60:63], v[232:239], v28\n"
      "s_mov_b64 exec, %[msk]\n"
      "buffer_load_dwordx4 v[56:59], v17, %[rws], %[so] offen offset:1024 sc1\n"
      "buffer_load_dwordx4 v[60:63], v17, %[rws], %[so] offen offset:1536 sc1\n"
      "s_mov_b64 exec, -1\n"
      "s_add_u32 %[so], %[so], 4096\n"
      "s_and_b32 %[so], %[so], 0x1ffff\n"
      "v_exp_f32 v210, v210\n"
      "v_exp_f32 v211, v211\n"
      "v_exp_f32 v212, v212\n"
      "v_mfma_f32_32x32x16_bf16 v[224:239], v[76:79], v[244:247], v[192:207]\n"
      "buffer_load_dwordx4 v[76:79], v16, %[rws], %[so] offen sc1\n"
      "v_exp_f32 v213, v213\n"
      "v_exp_f32 v214, v214\n"
      "v_exp_f32 v215, v215\n"
      "v_exp_f32 v216, v216\n"
      "v_exp_f32 v217, v217\n"
      "v_exp_f32 v218, v218\n"
      "v_smfmac_f32_32x32x32_bf16 v[144:159], v[80:83], v[208:215], v28\n"
      "s_buffer_load_dword %[dum], s[60:63], %[to1] offset:14336\n"
      "s_lshl_b32 %[tmp], %[wi], 11\n"
      "s_add_u32 m0, %[tmp], %[wm]\n"
      "s_lshl_b32 %[tmp], %[wi], 13\n"
      "buffer_load_dwordx4 v18, s[52:55], %[tmp] offen lds\n"
      "v_exp_f32 v219, v219\n"
      "v_exp_f32 v220, v220\n"
      "v_exp_f32 v221, v221\n"
      "v_exp_f32 v222, v222\n"
      "v_exp_f32 v223, v223\n"
      "v_exp_f32 v224, v224\n"
      "v_exp_f32 v225, v225\n"
      "v_smfmac_f32_32x32x32_bf16 v[144:159], v[84:87], v[216:223], v28\n"
      "v_exp_f32 v226, v226\n"
      "v_exp_f32 v227, v227\n"
      "v_exp_f32 v228, v228\n"
      "s_waitcnt vmcnt(18)\n"
      "v_mfma_f32_32x32x16_bf16 v[208:223], v[64:67], v[240:243], v[176:191]\n"
      "v_exp_f32 v229, v229\n"
      "v_exp_f32 v230, v230\n"
      "v_exp_f32 v231, v231\n"
      "v_exp_f32 v232, v232\n"
      "v_exp_f32 v233, v233\n"
      "v_exp_f32 v234, v234\n"
      "v_smfmac_f32_32x32x32_bf16 v[160:175], v[80:83], v[224:231], v28\n"
      "s_buffer_load_dword %[dum], s[60:63], %[to1] offset:17920\n"
      "v_exp_f32 v235, v235\n"
      "v_exp_f32 v236, v236\n"
      "v_exp_f32 v237, v237\n"
      "v_exp_f32 v238, v238\n"
      "v_exp_f32 v239, v239\n"
      "v_exp_f32 v208, v208\n"
      "v_exp_f32 v209, v209\n"
      "v_smfmac_f32_32x32x32_bf16 v[160:175], v[84:87], v[232:239], v28\n"
      "s_mov_b64 exec, %[msk]\n"
      "buffer_load_dwordx4 v[80:83], v17, %[rws], %[so] offen offset:1024 sc1\n"
      "buffer_load_dwordx4 v[84:87], v17, %[rws], %[so] offen offset:1536 sc1\n"
      "s_mov_b64 exec, -1\n"
      "s_add_u32 %[so], %[so], 4096\n"
      "s_and_b32 %[so], %[so], 0x1ffff\n"
      "v_exp_f32 v210, v210\n"
      "v_exp_f32 v211, v211\n"
      "v_exp_f32 v212, v212\n"
      "v_mfma_f32_32x32x16_bf16 v[224:239], v[64:67], v[244:247], v[192:207]\n"
      "buffer_load_dwordx4 v[64:67], v16, %[rws], %[so] offen sc1\n"
      "v_exp_f32 v213, v213\n"
      "v_exp_f32 v214, v214\n"
      "v_exp_f32 v215, v215\n"
      "v_exp_f32 v216, v216\n"
      "v_exp_f32 v217, v217\n"
      "v_exp_f32 v218, v218\n"
      "v_smfmac_f32_32x32x32_bf16 v[144:159], v[68:71], v[208:215], v28\n"
      "s_buffer_load_dword %[dum], s[60:63], %[to1] offset:21504\n"
      "s_lshl_b32 %[tmp], %[wi], 11\n"
      "s_add_u32 m0, %[tmp], %[wm]\n"
      "s_add_u32 m0, m0, 1024\n"
      "s_lshl_b32 %[tmp], %[wi], 13\n"
      "buffer_load_dwordx4 v19, s[52:55], %[tmp] offen lds\n"
      "s_add_u32 %[wi], %[wi], 1\n"
      "s_and_b32 %[wi], %[wi], 7\n"
      "v_exp_f32 v219, v219\n"
      "v_exp_f32 v220, v220\n"
      "v_exp_f32 v221, v221\n"
      "v_exp_f32 v222, v222\n"
      "v_exp_f32 v223, v223\n"
      "v_exp_f32 v224, v224\n"
      "v_exp_f32 v225, v225\n"
      "v_smfmac_f32_32x32x32_bf16 v[144:159], v[72:75], v[216:223], v28\n"
      "s_mov_b32 %[want], 0\n"
      "buffer_load_dwordx4 v[112:115], v20, %[rf0], %[want] offen\n"
      "s_add_u32 %[want], %[want], %[fs]\n"
      "v_exp_f32 v226, v226\n"
      "v_exp_f32 v227, v227\n"
      "v_exp_f32 v228, v228\n"
      "s_waitcnt vmcnt(19)\n"
      "v_mfma_f32_32x32x16_bf16 v[208:223], v[88:91], v[240:243], v[176:191]\n"
      "v_exp_f32 v229, v229\n"
      "v_exp_f32 v230, v230\n"
      "v_exp_f32 v231, v231\n"
      "v_exp_f32 v232, v232\n"
      "v_exp_f32 v233, v233\n"
      "v_exp_f32 v234, v234\n"
      "v_smfmac_f32_32x32x32_bf16 v[160:175], v[68:71], v[224:231], v28\n"
      "s_buffer_load_dword %[dum], s[60:63], %[to1] offset:25088\n"
      "v_exp_f32 v235, v235\n"
      "v_exp_f32 v236, v236\n"
      "v_exp_f32 v237, v237\n"
      "v_exp_f32 v238, v238\n"
      "v_exp_f32 v239, v239\n"
      "v_exp_f32 v208, v208\n"
      "v_exp_f32 v209, v209\n"
      "v_smfmac_f32_32x32x32_bf16 v[160:175], v[72:75], v[232:239], v28\n"
      "s_mov_b64 exec, %[msk]\n"
      "buffer_load_dwordx4 v[68:71], v17, %[rws], %[so] offen offset:1024 sc1\n"
      "buffer_load_dwordx4 v[72:75], v17, %[rws], %[so] offen offset:1536 sc1\n"
      "s_mov_b64 exec, -1\n"
      "s_add_u32 %[so], %[so], 4096\n"
      "s_and_b32 %[so], %[so], 0x1ffff\n"
      "v_exp_f32 v210, v210\n"
      "v_exp_f32 v211, v211\n"
      "v_exp_f32 v212, v212\n"
      "v_mfma_f32_32x32x16_bf16 v[224:239], v[88:91], v[244:247], v[192:207]\n"
      "buffer_load_dwordx4 v[88:91], v16, %[rws], %[so] offen sc1\n"
      "v_exp_f32 v213, v213\n"
      "v_exp_f32 v214, v214\n"
      "v_exp_f32 v215, v215\n"
      "v_exp_f32 v216, v216\n"
      "v_exp_f32 v217, v217\n"
      "v_exp_f32 v218, v218\n"
      "v_smfmac_f32_32x32x32_bf16 v[144:159], v[92:95], v[208:215], v28\n"
      "s_buffer_load_dword %[dum], s[60:63], %[to1] offset:28672\n"
      "s_lshl_b32 %[tmp], %[wi], 11\n"
      "s_add_u32 m0, %[tmp], %[wm]\n"
      "s_lshl_b32 %[tmp], %[wi], 13\n"
      "buffer_load_dwordx4 v18, s[52:55], %[tmp] offen lds\n"
      "v_exp_f32 v219, v219\n"
      "v_exp_f32 v220, v220\n"
      "v_exp_f32 v221, v221\n"
      "v_exp_f32 v222, v222\n"
      "v_exp_f32 v223, v223\n"
      "v_exp_f32 v224, v224\n"
      "v_exp_f32 v225, v225\n"
      "v_smfmac_f32_32x32x32_bf16 v[144:159], v[96:99], v[216:223], v28\n"
      "buffer_load_dwordx4 v[116:119], v20, %[rf0], %[want] offen\n"
      "s_add_u32 %[want], %[want], %[fs]\n"
      "v_exp_f32 v226, v226\n"
      "v_exp_f32 v227, v227\n"
      "v_exp_f32 v228, v228\n"
      "s_waitcnt vmcnt(20)\n"
      "v_mfma_f32_32x32x16_bf16 v[208:223], v[100:103], v[240:243], v[176:191]\n"
      "v_exp_f32 v229, v229\n"
      "v_exp_f32 v230, v230\n"
      "v_exp_f32 v231, v231\n"
      "v_exp_f32 v232, v232\n"
      "v_exp_f32 v233, v233\n"
      "v_exp_f32 v234, v234\n"
      "v_smfmac_f32_32x32x32_bf16 v[160:175], v[92:95], v[224:231], v28\n"
      "s_buffer_load_dword %[dum], s[60:63], %[to1] offset:32256\n"
      "v_exp_f32 v235, v235\n"
      "v_exp_f32 v236, v236\n"
      "v_exp_f32 v237, v237\n"
      "v_exp_f32 v238, v238\n"
      "v_exp_f32 v239, v239\n"
      "v_exp_f32 v208, v208\n"
      "v_exp_f32 v209, v209\n"
      "v_smfmac_f32_32x32x32_bf16 v[160:175], v[96:99], v[232:239], v28\n"
      "s_mov_b64 exec, %[msk]\n"
      "buffer_load_dwordx4 v[92:95], v17, %[rws], %[so] offen offset:1024 sc1\n"
      "buffer_load_dwordx4 v[96:99], v17, %[rws], %[so] offen offset:1536 sc1\n"
      "s_mov_b64 exec, -1\n"
      "s_add_u32 %[so], %[so], 4096\n"
      "s_and_b32 %[so], %[so], 0x1ffff\n"
      "v_exp_f32 v210, v210\n"
      "v_exp_f32 v211, v211\n"
      "v_exp_f32 v212, v212\n"
      "v_mfma_f32_32x32x16_bf16 v[224:239], v[100:103], v[244:247], v[192:207]\n"
      "buffer_load_dwordx4 v[100:103], v16, %[rws], %[so] offen sc1\n"
      "v_exp_f32 v213, v213\n"
      "v_exp_f32 v214, v214\n"
      "v_exp_f32 v215, v215\n"
      "v_exp_f32 v216, v216\n"
      "v_exp_f32 v217, v217\n"
      "v_exp_f32 v218, v218\n"
      "v_smfmac_f32_32x32x32_bf16 v[144:159], v[104:107], v[208:215], v28\n"
      "s_buffer_load_dword %[dum], s[60:63], %[to1] offset:35840\n"
      "s_lshl_b32 %[tmp], %[wi], 11\n"
      "s_add_u32 m0, %[tmp], %[wm]\n"
      "s_add_u32 m0, m0, 1024\n"
      "s_lshl_b32 %[tmp], %[wi], 13\n"
      "buffer_load_dwordx4 v19, s[52:55], %[tmp] offen lds\n"
      "s_add_u32 %[wi], %[wi], 1\n"
      "s_and_b32 %[wi], %[wi], 7\n"
      "v_exp_f32 v219, v219\n"
      "v_exp_f32 v220, v220\n"
      "v_exp_f32 v221, v221\n"
      "v_exp_f32 v222, v222\n"
      "v_exp_f32 v223, v223\n"
      "v_exp_f32 v224, v224\n"
      "v_exp_f32 v225, v225\n"
      "v_smfmac_f32_32x32x32_bf16 v[144:159], v[108:111], v[216:223], v28\n"
      "buffer_load_dwordx4 v[120:123], v20, %[rf0], %[want] offen\n"
      "s_add_u32 %[want], %[want], %[fs]\n"
      "v_exp_f32 v226, v226\n"
      "v_exp_f32 v227, v227\n"
      "v_exp_f32 v228, v228\n"
      "s_waitcnt vmcnt(21)\n"
      "v_mfma_f32_32x32x16_bf16 v[208:223], v[40:43], v[240:243], v[176:191]\n"
      "v_exp_f32 v229, v229\n"
      "v_exp_f32 v230, v230\n"
      "v_exp_f32 v231, v231\n"
      "v_exp_f32 v232, v232\n"
      "v_exp_f32 v233, v233\n"
      "v_exp_f32 v234, v234\n"
      "v_smfmac_f32_32x32x32_bf16 v[160:175], v[104:107], v[224:231], v28\n"
      "s_buffer_load_dword %[dum], s[60:63], %[to1] offset:39424\n"
      "v_exp_f32 v235, v235\n"
      "v_exp_f32 v236, v236\n"
      "v_exp_f32 v237, v237\n"
      "v_exp_f32 v238, v238\n"
      "v_exp_f32 v239, v239\n"
      "v_exp_f32 v208, v208\n"
      "v_exp_f32 v209, v209\n"
      "v_smfmac_f32_32x32x32_bf16 v[160:175], v[108:111], v[232:239], v28\n"
      "s_mov_b64 exec, %[msk]\n"
      "buffer_load_dwordx4 v[104:107], v17, %[rws], %[so] offen offset:1024 sc1\n"
      "buffer_load_dwordx4 v[108:111], v17, %[rws], %[so] offen offset:1536 sc1\n"
      "s_mov_b64 exec, -1\n"
      "s_add_u32 %[so], %[so], 4096\n"
      "s_and_b32 %[so], %[so], 0x1ffff\n"
      "v_exp_f32 v210, v210\n"
      "v_exp_f32 v211, v211\n"
      "v_exp_f32 v212, v212\n"
      "v_mfma_f32_32x32x16_bf16 v[224:239], v[40:43], v[244:247], v[192:207]\n"
      "buffer_load_dwordx4 v[40:43], v16, %[rws], %[so] offen sc1\n"
      "v_exp_f32 v213, v213\n"
      "v_exp_f32 v214, v214\n"
      "v_exp_f32 v215, v215\n"
      "v_exp_f32 v216, v216\n"
      "v_exp_f32 v217, v217\n"
      "v_exp_f32 v218, v218\n"
      "v_smfmac_f32_32x32x32_bf16 v[144:159], v[44:47], v[208:215], v28\n"
      "s_buffer_load_dword %[dum], s[60:63], %[to1] offset:43008\n"
      "s_lshl_b32 %[tmp], %[wi], 11\n"
      "s_add_u32 m0, %[tmp], %[wm]\n"
      "s_lshl_b32 %[tmp], %[wi], 13\n"
      "buffer_load_dwordx4 v18, s[52:55], %[tmp] offen lds\n"
      "v_exp_f32 v219, v219\n"
      "v_exp_f32 v220, v220\n"
      "v_exp_f32 v221, v221\n"
      "v_exp_f32 v222, v222\n"
      "v_exp_f32 v223, v223\n"
      "v_exp_f32 v224, v224\n"
      "v_exp_f32 v225, v225\n"
      "v_smfmac_f32_32x32x32_bf16 v[144:159], v[48:51], v[216:223], v28\n"
      "v_exp_f32 v226, v226\n"
      "v_exp_f32 v227, v227\n"
      "v_exp_f32 v228, v228\n"
      "s_waitcnt vmcnt(21)\n"
      "v_mfma_f32_32x32x16_bf16 v[208:223], v[52:55], v[240:243], v[176:191]\n"
      "v_exp_f32 v229, v229\n"
      "v_exp_f32 v230, v230\n"
      "v_exp_f32 v231, v231\n"
      "v_exp_f32 v232, v232\n"
      "v_exp_f32 v233, v233\n"
      "v_exp_f32 v234, v234\n"
      "v_smfmac_f32_32x32x32_bf16 v[160:175], v[44:47], v[224:231], v28\n"
      "s_buffer_load_dword %[dum], s[60:63], %[to1] offset:46592\n"
      "v_exp_f32 v235, v235\n"
      "v_exp_f32 v236, v236\n"
      "v_exp_f32 v237, v237\n"
      "v_exp_f32 v238, v238\n"
      "v_exp_f32 v239, v239\n"
      "v_exp_f32 v208, v208\n"
      "v_exp_f32 v209, v209\n"
      "v_smfmac_f32_32x32x32_bf16 v[160:175], v[48:51], v[232:239], v28\n"
      "s_mov_b64 exec, %[msk]\n"
      "buffer_load_dwordx4 v[44:47], v17, %[rws], %[so] offen offset:1024 sc1\n"
      "buffer_load_dwordx4 v[48:51], v17, %[rws], %[so] offen offset:1536 sc1\n"
      "s_mov_b64 exec, -1\n"
      "s_add_u32 %[so], %[so], 4096\n"
      "s_and_b32 %[so], %[so], 0x1ffff\n"
      "v_exp_f32 v210, v210\n"
      "v_exp_f32 v211, v211\n"
      "v_exp_f32 v212, v212\n"
      "v_mfma_f32_32x32x16_bf16 v[224:239], v[52:55], v[244:247], v[192:207]\n"
      "buffer_load_dwordx4 v[52:55], v16, %[rws], %[so] offen sc1\n"
      "v_exp_f32 v213, v213\n"
      "v_exp_f32 v214, v214\n"
      "v_exp_f32 v215, v215\n"
      "v_exp_f32 v216, v216\n"
      "v_exp_f32 v217, v217\n"
      "v_exp_f32 v218, v218\n"
      "v_smfmac_f32_32x32x32_bf16 v[144:159], v[56:59], v[208:215], v28\n"
      "s_buffer_load_dword %[dum], s[60:63], %[to1] offset:50176\n"
      "s_lshl_b32 %[tmp], %[wi], 11\n"
      "s_add_u32 m0, %[tmp], %[wm]\n"
      "s_add_u32 m0, m0, 1024\n"
      "s_lshl_b32 %[tmp], %[wi], 13\n"
      "buffer_load_dwordx4 v19, s[52:55], %[tmp] offen lds\n"
      "s_add_u32 %[wi], %[wi], 1\n"
      "s_and_b32 %[wi], %[wi], 7\n"
      "v_exp_f32 v219, v219\n"
      "v_exp_f32 v220, v220\n"
      "v_exp_f32 v221, v221\n"
      "v_exp_f32 v222, v222\n"
      "v_exp_f32 v223, v223\n"
      "v_exp_f32 v224, v224\n"
      "v_exp_f32 v225, v225\n"
      "v_smfmac_f32_32x32x32_bf16 v[144:159], v[60:63], v[216:223], v28\n"
      "buffer_load_dwordx4 v[124:127], v20, %[rf0], %[want] offen\n"
      "s_add_u32 %[want], %[want], %[fs]\n"
      "v_exp_f32 v226, v226\n"
      "v_exp_f32 v227, v227\n"
      "v_exp_f32 v228, v228\n"
      "s_waitcnt vmcnt(22)\n"
      "v_mfma_f32_32x32x16_bf16 v[208:223], v[76:79], v[240:243], v[176:191]\n"
      "v_exp_f32 v229, v229\n"
      "v_exp_f32 v230, v230\n"
      "v_exp_f32 v231, v231\n"
      "v_exp_f32 v232, v232\n"
      "v_exp_f32 v233, v233\n"
      "v_exp_f32 v234, v234\n"
      "v_smfmac_f32_32x32x32_bf16 v[160:175], v[56:59], v[224:231], v28\n"
      "s_buffer_load_dword %[dum], s[60:63], %[to1] offset:53760\n"
      "v_exp_f32 v235, v235\n"
      "v_exp_f32 v236, v236\n"
      "v_exp_f32 v237, v237\n"
      "v_exp_f32 v238, v238\n"
      "v_exp_f32 v239, v239\n"
      "v_exp_f32 v208, v208\n"
      "v_exp_f32 v209, v209\n"
      "v_smfmac_f32_32x32x32_bf16 v[160:175], v[60:63], v[232:239], v28\n"
      "s_mov_b64 exec, %[msk]\n"
      "buffer_load_dwordx4 v[56:59], v17, %[rws], %[so] offen offset:1024 sc1\n"
      "buffer_load_dwordx4 v[60:63], v17, %[rws], %[so] offen offset:1536 sc1\n"
      "s_mov_b64 exec, -1\n"
      "s_add_u32 %[so], %[so], 4096\n"
      "s_and_b32 %[so], %[so], 0x1ffff\n"
      "v_exp_f32 v210, v210\n"
      "v_exp_f32 v211, v211\n"
      "v_exp_f32 v212, v212\n"
      "v_mfma_f32_32x32x16_bf16 v[224:239], v[76:79], v[244:247], v[192:207]\n"
      "v_exp_f32 v213, v213\n"
      "v_exp_f32 v214, v214\n"
      "v_exp_f32 v215, v215\n"
      "v_exp_f32 v216, v216\n"
      "v_exp_f32 v217, v217\n"
      "v_exp_f32 v218, v218\n"
      "v_smfmac_f32_32x32x32_bf16 v[144:159], v[80:83], v[208:215], v28\n"
      "s_lshl_b32 %[tmp], %[wi], 11\n"
      "s_add_u32 m0, %[tmp], %[wm]\n"
      "s_lshl_b32 %[tmp], %[wi], 13\n"
      "buffer_load_dwordx4 v18, s[52:55], %[tmp] offen lds\n"
      "v_exp_f32 v219, v219\n"
      "v_exp_f32 v220, v220\n"
      "v_exp_f32 v221, v221\n"
      "v_exp_f32 v222, v222\n"
      "v_exp_f32 v223, v223\n"
      "v_exp_f32 v224, v224\n"
      "v_exp_f32 v225, v225\n"
      "v_smfmac_f32_32x32x32_bf16 v[144:159], v[84:87], v[216:223], v28\n"
      "v_exp_f32 v226, v226\n"
      "v_exp_f32 v227, v227\n"
      "v_exp_f32 v228, v228\n"
      "s_waitcnt vmcnt(20)\n"
      "v_mfma_f32_32x32x16_bf16 v[208:223], v[64:67], v[240:243], v[176:191]\n"
      "v_exp_f32 v229, v229\n"
      "v_exp_f32 v230, v230\n"
      "v_exp_f32 v231, v231\n"
      "v_exp_f32 v232, v232\n"
      "v_exp_f32 v233, v233\n"
      "v_exp_f32 v234, v234\n"
      "v_smfmac_f32_32x32x32_bf16 v[160:175], v[80:83], v[224:231], v28\n"
      "v_exp_f32 v235, v235\n"
      "v_exp_f32 v236, v236\n"
      "v_exp_f32 v237, v237\n"
      "v_exp_f32 v238, v238\n"
      "v_exp_f32 v239, v239\n"
      "v_exp_f32 v208, v208\n"
      "v_exp_f32 v209, v209\n"
      "v_smfmac_f32_32x32x32_bf16 v[160:175], v[84:87], v[232:239], v28\n"
      "s_setprio 1\n"
      "v_exp_f32 v210, v210\n"
      "v_exp_f32 v211, v211\n"
      "v_exp_f32 v212, v212\n"
      "v_mfma_f32_32x32x16_bf16 v[224:239], v[64:67], v[244:247], v[192:207]\n"
      "v_exp_f32 v213, v213\n"
      "v_exp_f32 v214, v214\n"
      "v_exp_f32 v215, v215\n"
      "v_exp_f32 v216, v216\n"
      "v_exp_f32 v217, v217\n"
      "v_exp_f32 v218, v218\n"
      "v_smfmac_f32_32x32x32_bf16 v[144:159], v[68:71], v[208:215], v28\n"
      "s_lshl_b32 %[tmp], %[wi], 11\n"
      "s_add_u32 m0, %[tmp], %[wm]\n"
      "s_add_u32 m0, m0, 1024\n"
      "s_lshl_b32 %[tmp], %[wi], 13\n"
      "buffer_load_dwordx4 v19, s[52:55], %[tmp] offen lds\n"
      "s_add_u32 %[wi], %[wi], 1\n"
      "s_and_b32 %[wi], %[wi], 7\n"
      "v_exp_f32 v219, v219\n"
      "v_exp_f32 v220, v220\n"
      "v_exp_f32 v221, v221\n"
      "v_exp_f32 v222, v222\n"
      "v_exp_f32 v223, v223\n"
      "v_exp_f32 v224, v224\n"
      "v_exp_f32 v225, v225\n"
      "v_smfmac_f32_32x32x32_bf16 v[144:159], v[72:75], v[216:223], v28\n"
      "s_mov_b32 %[want], %[fo2]\n"
      "buffer_load_dwordx4 v[128:131], v20, %[rf1], %[want] offen\n"
      "s_add_u32 %[want], %[want], %[fs]\n"
      "v_exp_f32 v226, v226\n"
      "v_exp_f32 v227, v227\n"
      "v_exp_f32 v228, v228\n"
      "s_waitcnt vmcnt(17)\n"
      "v_mfma_f32_32x32x16_bf16 v[208:223], v[88:91], v[240:243], v[176:191]\n"
      "v_exp_f32 v229, v229\n"
      "v_exp_f32 v230, v230\n"
      "v_exp_f32 v231, v231\n"
      "v_exp_f32 v232, v232\n"
      "v_exp_f32 v233, v233\n"
      "v_exp_f32 v234, v234\n"
      "v_smfmac_f32_32x32x32_bf16 v[160:175], v[68:71], v[224:231], v28\n"
      "v_exp_f32 v235, v235\n"
      "v_exp_f32 v236, v236\n"
      "v_exp_f32 v237, v237\n"
      "v_exp_f32 v238, v238\n"
      "v_exp_f32 v239, v239\n"
      "v_exp_f32 v208, v208\n"
      "v_exp_f32 v209, v209\n"
      "v_smfmac_f32_32x32x32_bf16 v[160:175], v[72:75], v[232:239], v28\n"
      "v_exp_f32 v210, v210\n"
      "v_exp_f32 v211, v211\n"
      "v_exp_f32 v212, v212\n"
      "v_mfma_f32_32x32x16_bf16 v[224:239], v[88:91], v[244:247], v[192:207]\n"
      "v_exp_f32 v213, v213\n"
      "v_exp_f32 v214, v214\n"
      "v_exp_f32 v215, v215\n"
      "v_exp_f32 v216, v216\n"
      "v_exp_f32 v217, v217\n"
      "v_exp_f32 v218, v218\n"
      "v_smfmac_f32_32x32x32_bf16 v[144:159], v[92:95], v[208:215], v28\n"
      "v_exp_f32 v219, v219\n"
      "v_exp_f32 v220, v220\n"
      "v_exp_f32 v221, v221\n"
      "v_exp_f32 v222, v222\n"
      "v_exp_f32 v223, v223\n"
      "v_exp_f32 v224, v224\n"
      "v_exp_f32 v225, v225\n"
      "v_smfmac_f32_32x32x32_bf16 v[144:159], v[96:99], v[216:223], v28\n"
      "buffer_load_dwordx4 v[132:135], v20, %[rf1], %[want] offen\n"
      "s_add_u32 %[want], %[want], %[fs]\n"
      "v_exp_f32 v226, v226\n"
      "v_exp_f32 v227, v227\n"
      "v_exp_f32 v228, v228\n"
      "s_waitcnt vmcnt(13)\n"
      "v_mfma_f32_32x32x16_bf16 v[208:223], v[100:103], v[240:243], v[176:191]\n"
      "v_exp_f32 v229, v229\n"
      "v_exp_f32 v230, v230\n"
      "v_exp_f32 v231, v231\n"
      "v_exp_f32 v232, v232\n"
      "v_exp_f32 v233, v233\n"
      "v_exp_f32 v234, v234\n"
      "v_smfmac_f32_32x32x32_bf16 v[160:175], v[92:95], v[224:231], v28\n"
      "v_exp_f32 v235, v235\n"
      "v_exp_f32 v236, v236\n"
      "v_exp_f32 v237, v237\n"
      "v_exp_f32 v238, v238\n"
      "v_exp_f32 v239, v239\n"
      "v_exp_f32 v208, v208\n"
      "v_exp_f32 v209, v209\n"
      "v_smfmac_f32_32x32x32_bf16 v[160:175], v[96:99], v[232:239], v28\n"
      "v_exp_f32 v210, v210\n"
      "v_exp_f32 v211, v211\n"
      "v_exp_f32 v212, v212\n"
      "v_mfma_f32_32x32x16_bf16 v[224:239], v[100:103], v[244:247], v[192:207]\n"
      "v_exp_f32 v213, v213\n"
      "v_exp_f32 v214, v214\n"
      "v_exp_f32 v215, v215\n"
      "v_exp_f32 v216, v216\n"
      "v_exp_f32 v217, v217\n"
      "v_exp_f32 v218, v218\n"
      "v_smfmac_f32_32x32x32_bf16 v[144:159], v[104:107], v[208:215], v28\n"
      "v_exp_f32 v219, v219\n"
      "v_exp_f32 v220, v220\n"
      "v_exp_f32 v221, v221\n"
      "v_exp_f32 v222, v222\n"
      "v_exp_f32 v223, v223\n"
      "v_exp_f32 v224, v224\n"
      "v_exp_f32 v225, v225\n"
      "v_smfmac_f32_32x32x32_bf16 v[144:159], v[108:111], v[216:223], v28\n"
      "buffer_load_dwordx4 v[136:139], v20, %[rf1], %[want] offen\n"
      "s_add_u32 %[want], %[want], %[fs]\n"
      "buffer_load_dwordx2 v[32:33], v23, %[rws], 0 offen sc1\n"
      "v_exp_f32 v226, v226\n"
      "v_exp_f32 v227, v227\n"
      "v_exp_f32 v228, v228\n"
      "s_waitcnt vmcnt(11)\n"
      "v_mfma_f32_32x32x16_bf16 v[208:223], v[40:43], v[240:243], v[176:191]\n"
      "v_exp_f32 v229, v229\n"
      "v_exp_f32 v230, v230\n"
      "v_exp_f32 v231, v231\n"
      "v_exp_f32 v232, v232\n"
      "v_exp_f32 v233, v233\n"
      "v_exp_f32 v234, v234\n"
      "v_smfmac_f32_32x32x32_bf16 v[160:175], v[104:107], v[224:231], v28\n"
      "v_exp_f32 v235, v235\n"
      "v_exp_f32 v236, v236\n"
      "v_exp_f32 v237, v237\n"
      "v_exp_f32 v238, v238\n"
      "v_exp_f32 v239, v239\n"
      "v_exp_f32 v208, v208\n"
      "v_exp_f32 v209, v209\n"
      "v_smfmac_f32_32x32x32_bf16 v[160:175], v[108:111], v[232:239], v28\n"
      "v_exp_f32 v210, v210\n"
      "v_exp_f32 v211, v211\n"
      "v_exp_f32 v212, v212\n"
      "v_mfma_f32_32x32x16_bf16 v[224:239], v[40:43], v[244:247], v[192:207]\n"
      "v_exp_f32 v213, v213\n"
      "v_exp_f32 v214, v214\n"
      "v_exp_f32 v215, v215\n"
      "v_exp_f32 v216, v216\n"
      "v_exp_f32 v217, v217\n"
      "v_exp_f32 v218, v218\n"
      "v_smfmac_f32_32x32x32_bf16 v[144:159], v[44:47], v[208:215], v28\n"
      "v_exp_f32 v219, v219\n"
      "v_exp_f32 v220, v220\n"
      "v_exp_f32 v221, v221\n"
      "v_exp_f32 v222, v222\n"
      "v_exp_f32 v223, v223\n"
      "v_exp_f32 v224, v224\n"
      "v_exp_f32 v225, v225\n"
      "v_smfmac_f32_32x32x32_bf16 v[144:159], v[48:51], v[216:223], v28\n"
      "buffer_load_dwordx4 v[140:143], v20, %[rf1], %[want] offen\n"
      "s_add_u32 %[want], %[want], %[fs]\n"
      "v_exp_f32 v226, v226\n"
      "v_exp_f32 v227, v227\n"
      "v_exp_f32 v228, v228\n"
      "s_waitcnt vmcnt(7)\n"
      "v_mfma_f32_32x32x16_bf16 v[208:223], v[52:55], v[240:243], v[176:191]\n"
      "v_exp_f32 v229, v229\n"
      "v_exp_f32 v230, v230\n"
      "v_exp_f32 v231, v231\n"
      "v_exp_f32 v232, v232\n"
      "v_exp_f32 v233, v233\n"
      "v_exp_f32 v234, v234\n"
      "v_smfmac_f32_32x32x32_bf16 v[160:175], v[44:47], v[224:231], v28\n"
      "v_exp_f32 v235, v235\n"
      "v_exp_f32 v236, v236\n"
      "v_exp_f32 v237, v237\n"
      "v_exp_f32 v238, v238\n"
      "v_exp_f32 v239, v239\n"
      "v_exp_f32 v208, v208\n"
      "v_exp_f32 v209, v209\n"
      "v_smfmac_f32_32x32x32_bf16 v[160:175], v[48:51], v[232:239], v28\n"
      "s_setprio 0\n"
      "v_exp_f32 v210, v210\n"
      "v_exp_f32 v211, v211\n"
      "v_exp_f32 v212, v212\n"
      "v_mfma_f32_32x32x16_bf16 v[224:239], v[52:55], v[244:247], v[192:207]\n"
      "v_exp_f32 v213, v213\n"
      "v_exp_f32 v214, v214\n"
      "v_exp_f32 v215, v215\n"
      "v_exp_f32 v216, v216\n"
      "v_exp_f32 v217, v217\n"
      "v_exp_f32 v218, v218\n"
      "v_smfmac_f32_32x32x32_bf16 v[144:159], v[56:59], v[208:215], v28\n"
      "v_exp_f32 v219, v219\n"
      "v_exp_f32 v220, v220\n"
      "v_exp_f32 v221, v221\n"
      "v_exp_f32 v222, v222\n"
      "v_exp_f32 v223, v223\n"
      "v_exp_f32 v224, v224\n"
      "v_exp_f32 v225, v225\n"
      "v_smfmac_f32_32x32x32_bf16 v[144:159], v[60:63], v[216:223], v28\n"
      "v_exp_f32 v226, v226\n"
      "v_exp_f32 v227, v227\n"
      "v_exp_f32 v228, v228\n"
      "v_exp_f32 v229, v229\n"
      "v_exp_f32 v230, v230\n"
      "v_exp_f32 v231, v231\n"
      "v_exp_f32 v232, v232\n"
      "v_exp_f32 v233, v233\n"
      "v_exp_f32 v234, v234\n"
      "v_smfmac_f32_32x32x32_bf16 v[160:175], v[56:59], v[224:231], v28\n"
      "s_nop 3\n"
      "ds_write_b128 %[cmb], v[144:147]\n"
      "ds_write_b128 %[cmb], v[148:151] offset:1024\n"
      "ds_write_b128 %[cmb], v[152:155] offset:2048\n"
      "v_exp_f32 v235, v235\n"
      "v_exp_f32 v236, v236\n"
      "v_exp_f32 v237, v237\n"
      "v_exp_f32 v238, v238\n"
      "v_exp_f32 v239, v239\n"
      "s_nop 1\n"
      "v_smfmac_f32_32x32x32_bf16 v[160:175], v[60:63], v[232:239], v28\n"
      "s_waitcnt vmcnt(0) lgkmcnt(0)\n"
      "s_nop 11\n"
      "99:\n"
        : "=&{v[160:175]}"(o), "+{v[40:43]}"(rk), "+{v[44:47]}"(rv0), "+{v[48:51]}"(rv1),
          "=&{v[112:127]}"(ffa), "=&{v[128:143]}"(ffb), [so] "+s"(so), [wi] "+s"(wi),
          "=&{v[34:35]}"(xres0), "=&{v[36:37]}"(xres1), "=&{v[12:15]}"(wr4), "=&{v[32:33]}"(rfl2),
          [want] "=&s"(want), [tmp] "=&s"(tmp), [rdy] "+s"(rdy), [cnt] "=&s"(cnt), [ret] "=&s"(ret), [dum] "=&s"(dum)
        : "{v[252:255]}"(k0), "{v[240:243]}"(qa), "{v[244:247]}"(qb), "{v[16:19]}"(va0), "{v[20:23]}"(va1), "{v[24:27]}"(va2),
          [rx] "s"(rx), [rws] "s"(rws), "{s[52:67]}"(rw4), [rwr] "s"(rwr), [rf0] "s"(rf0),
          [rf1] "s"(rf1), [rfl] "s"(rfl), [msk] "s"(0x0000FFFF0000FFFFull), [fs] "s"(fs), [fo2] "s"(fo2), [sob] "s"(sob), [srf] "s"(srf),
          [tag] "s"(tag), [to0] "s"(to0), [to1] "s"(to1), [to2] "s"(to2), [wid] "s"(w), [wm] "s"(wm), [cmb] "v"(lb + L_CMB + (w ^ 4) * 3072 + lane * 16),
          [tc] "s"(min((xi & 15) * 8 + w, TCODE - 1) * 64)
        : "memory", "scc", "vcc", "v28", "v248", "v249", "v250", "v251",
        "v52", "v53", "v54", "v55", "v56", "v57", "v58", "v59", "v60", "v61", "v62", "v63",
        "v64", "v65", "v66", "v67", "v68", "v69", "v70", "v71", "v72", "v73", "v74", "v75",
        "v76", "v77", "v78", "v79", "v80", "v81", "v82", "v83", "v84", "v85", "v86", "v87",
        "v88", "v89", "v90", "v91", "v92", "v93", "v94", "v95", "v96", "v97", "v98", "v99",
        "v100", "v101", "v102", "v103", "v104", "v105", "v106", "v107", "v108", "v109", "v110", "v111",
        "v144", "v145", "v146", "v147", "v148", "v149", "v150", "v151", "v152", "v153", "v154", "v155",
        "v156", "v157", "v158", "v159", "v176", "v177", "v178", "v179", "v180", "v181", "v182", "v183",
        "v184", "v185", "v186", "v187", "v188", "v189", "v190", "v191", "v192", "v193", "v194", "v195",
        "v196", "v197", "v198", "v199", "v200", "v201", "v202", "v203", "v204", "v205", "v206", "v207",
        "v208", "v209", "v210", "v211", "v212", "v213", "v214", "v215", "v216", "v217", "v218", "v219",
        "v220", "v221", "v222", "v223", "v224", "v225", "v226", "v227", "v228", "v229", "v230", "v231",
        "v232", "v233", "v234", "v235", "v236", "v237", "v238", "v239"
    );
  }
  RELANE();
  if (__builtin_expect(!__all((rfl2.x == (u32)tag) & (rfl2.y == (u32)(tag >> 32))), 0)) {
#pragma unroll 1
    for (u32 spin = 0; spin < SPIN_CAP; ++spin) {
      __builtin_amdgcn_s_sleep(2);
      rfl2 = __builtin_amdgcn_raw_buffer_load_b64(rs, WS_RF + (panel * 8 + w) * 128 + (lane & 3) * 8, 0, AUX_SC1);
      if (__all((rfl2.x == (u32)tag) & (rfl2.y == (u32)(tag >> 32)))) break;
    }
  }
  lds_barrier();
  {
    const u32 pb = L_CMB + w * 3072;
    const f32x4 a = *(LAS f32x4*)(L + pb + lane * 16), c = *(LAS f32x4*)(L + pb + 1024 + lane * 16);
    o[8] += *(LAS float*)(L + pb + 2048 + lane * 16);
    float asum = 0.f;
#pragma unroll
    for (int i = 0; i < 4; ++i) {
      o[i] += a[i];
      o[4 + i] += c[i];
      asum += fabsf(o[i]) + fabsf(o[4 + i]);
    }
    const bool safe = (o[8] < 1.0995116e12f) & (o[8] > 9.094947e-13f) & (asum < 3.0e38f);
    auto publish = [&](const f32x16& v) {
      const float rl = __builtin_amdgcn_rcpf(v[8]);
      const u32x4 ow = {pack2(v[0] * rl, v[1] * rl), pack2(v[2] * rl, v[3] * rl), pack2(v[4] * rl, v[5] * rl), pack2(v[6] * rl, v[7] * rl)};
      __builtin_amdgcn_raw_buffer_store_b128(clampw4(ow), rs, sob + lane * 16, 0, AUX_SC1);
    };
    if (__builtin_expect(__all(safe), 1)) {
      publish(o);
    } else {
      const u32x4 qf = qb;
      const u32 kb = WS_KV + (4 * hg + j) * 131072;
      const f32x16 z = {};
      float m = -INFINITY;
#pragma unroll 1
      for (int t = 0; t < 64; ++t)
        m = fmaxf(m, max16x(mfma32(__builtin_amdgcn_raw_buffer_load_b128(rs, kb + t * 2048 + lane * 16, 0, AUX_SC1), qf, z)));
      const f32x16 negm = bcast16(-max32(m));
      o = z;
#pragma unroll 1
      for (int t = 0; t < 64; ++t) {
        const u32x4 kf = __builtin_amdgcn_raw_buffer_load_b128(rs, kb + t * 2048 + lane * 16, 0, AUX_SC1);
        u32x4 v0 = __builtin_amdgcn_raw_buffer_load_b128(rs, kb + t * 2048 + 1024 + hh * 256 + l15 * 16, 0, AUX_SC1);
        u32x4 v1 = __builtin_amdgcn_raw_buffer_load_b128(rs, kb + t * 2048 + 1536 + hh * 256 + l15 * 16, 0, AUX_SC1);
        if (lane & 16) v0 = v1 = ones4;
        const f32x16 s = mfma32(kf, qf, negm);
        f32x8 p0, p1;
#pragma unroll
        for (int i = 0; i < 8; ++i) {
          p0[i] = __builtin_amdgcn_exp2f(s[i]);
          p1[i] = __builtin_amdgcn_exp2f(s[8 + i]);
        }
        o = smfmac(v0, p0, o);
        o = smfmac(v1, p1, o);
      }
      asm volatile("; true maximum" : "+v"(o));
      publish(o);
    }
    asm volatile("" ::: "memory");
  }

  V4 of;
  auto load_o = [&](V4& d) {
#pragma unroll
    for (int i = 0; i < 4; ++i) d.v[i] = __builtin_amdgcn_raw_buffer_load_b128(rs, WS_OB + (panel * 32 + 4 * w + i) * 1024 + lane * 16, 0, AUX_SC1);
  };
  u32x16 wof[4];
  {
    const u32 row = 16 * (g >> 1) + 4 * (g & 1) + ((lane >> 2) & 3);
    const u32 a = L_WO + row * 256 + ((w ^ (row & 7)) * 32) + (lane & 3) * 8;
#pragma unroll
    for (int i = 0; i < 16; ++i) {
      const u32x4 f = ldtr2(L, a + i * 8192, a + i * 8192 + 2048);
#pragma unroll
      for (int e = 0; e < 4; ++e) wof[i >> 2][4 * (i & 3) + e] = f[e];
    }
    asm volatile("" : "+{v[144:159]}"(wof[0]), "+{v[160:175]}"(wof[1]), "+{v[176:191]}"(wof[2]), "+{v[192:207]}"(wof[3]));
  }
  float xs[2][4] = {{lo16(xres0.x), hi16(xres0.x), lo16(xres0.y), hi16(xres0.y)}, {lo16(xres1.x), hi16(xres1.x), lo16(xres1.y), hi16(xres1.y)}};
  float t4 = dot2(wr4.w, ONES, dot2(wr4.z, ONES, dot2(wr4.y, ONES, dot2(wr4.x, ONES, 0.f))));
  u32 oa_ = lb + L_OI + (g >> 1) * 1024 + ((g & 1) * 32 + l15) * 16, hta = L_HT + l31 * 272 + w * 32 + hh * 16;
  asm volatile("" : "+v"(xs[0][0]), "+v"(xs[0][1]), "+v"(xs[0][2]), "+v"(xs[0][3]), "+v"(xs[1][0]), "+v"(xs[1][1]), "+v"(xs[1][2]),
                    "+v"(xs[1][3]), "+v"(t4), "+v"(oa_), "+v"(hta));
  lds_barrier();
  {
    poll(of, load_o,
          [&](const V4& d) { return !__any(max(max(max4(d.v[0]), max4(d.v[1])), max(max4(d.v[2]), max4(d.v[3]))) == SENT); });
#pragma unroll
    for (int i = 0; i < 4; ++i) sts128(L, L_OI + (4 * w + i) * 1024 + lane * 16, of.v[i]);
  }
  lds_barrier();
  {
    u32x2 h2[2];
    f32x4 a0, a1;
    asm volatile(
      "ds_read_b128 v[208:211], %[oa] offset:0\n"
      "ds_read_b128 v[212:215], %[oa] offset:256\n"
      "ds_read_b128 v[216:219], %[oa] offset:2048\n"
      "ds_read_b128 v[220:223], %[oa] offset:2304\n"
      "ds_read_b128 v[224:227], %[oa] offset:4096\n"
      "ds_read_b128 v[228:231], %[oa] offset:4352\n"
      "ds_read_b128 v[232:235], %[oa] offset:6144\n"
      "ds_read_b128 v[236:239], %[oa] offset:6400\n"
      "ds_read_b128 v[240:243], %[oa] offset:8192\n"
      "ds_read_b128 v[244:247], %[oa] offset:8448\n"
      "ds_read_b128 v[248:251], %[oa] offset:10240\n"
      "ds_read_b128 v[252:255], %[oa] offset:10496\n"
      "s_waitcnt lgkmcnt(11)\n"
      "v_mfma_f32_16x16x32_bf16 v[64:67], v[144:147], v[208:211], 0\n"
      "ds_read_b128 v[208:211], %[oa] offset:12288\n"
      "s_waitcnt lgkmcnt(11)\n"
      "v_mfma_f32_16x16x32_bf16 v[68:71], v[144:147], v[212:215], 0\n"
      "ds_read_b128 v[212:215], %[oa] offset:12544\n"
      "s_waitcnt lgkmcnt(11)\n"
      "v_mfma_f32_16x16x32_bf16 v[64:67], v[148:151], v[216:219], v[64:67]\n"
      "ds_read_b128 v[216:219], %[oa] offset:14336\n"
      "s_waitcnt lgkmcnt(11)\n"
      "v_mfma_f32_16x16x32_bf16 v[68:71], v[148:151], v[220:223], v[68:71]\n"
      "ds_read_b128 v[220:223], %[oa] offset:14592\n"
      "s_waitcnt lgkmcnt(11)\n"
      "v_mfma_f32_16x16x32_bf16 v[64:67], v[152:155], v[224:227], v[64:67]\n"
      "ds_read_b128 v[224:227], %[oa] offset:16384\n"
      "s_waitcnt lgkmcnt(11)\n"
      "v_mfma_f32_16x16x32_bf16 v[68:71], v[152:155], v[228:231], v[68:71]\n"
      "ds_read_b128 v[228:231], %[oa] offset:16640\n"
      "s_waitcnt lgkmcnt(11)\n"
      "v_mfma_f32_16x16x32_bf16 v[64:67], v[156:159], v[232:235], v[64:67]\n"
      "ds_read_b128 v[232:235], %[oa] offset:18432\n"
      "s_waitcnt lgkmcnt(11)\n"
      "v_mfma_f32_16x16x32_bf16 v[68:71], v[156:159], v[236:239], v[68:71]\n"
      "ds_read_b128 v[236:239], %[oa] offset:18688\n"
      "s_waitcnt lgkmcnt(11)\n"
      "v_mfma_f32_16x16x32_bf16 v[64:67], v[160:163], v[240:243], v[64:67]\n"
      "ds_read_b128 v[240:243], %[oa] offset:20480\n"
      "s_waitcnt lgkmcnt(11)\n"
      "v_mfma_f32_16x16x32_bf16 v[68:71], v[160:163], v[244:247], v[68:71]\n"
      "ds_read_b128 v[244:247], %[oa] offset:20736\n"
      "s_waitcnt lgkmcnt(11)\n"
      "v_mfma_f32_16x16x32_bf16 v[64:67], v[164:167], v[248:251], v[64:67]\n"
      "ds_read_b128 v[248:251], %[oa] offset:22528\n"
      "s_waitcnt lgkmcnt(11)\n"
      "v_mfma_f32_16x16x32_bf16 v[68:71], v[164:167], v[252:255], v[68:71]\n"
      "ds_read_b128 v[252:255], %[oa] offset:22784\n"
      "s_waitcnt lgkmcnt(11)\n"
      "v_mfma_f32_16x16x32_bf16 v[64:67], v[168:171], v[208:211], v[64:67]\n"
      "ds_read_b128 v[208:211], %[oa] offset:24576\n"
      "s_waitcnt lgkmcnt(11)\n"
      "v_mfma_f32_16x16x32_bf16 v[68:71], v[168:171], v[212:215], v[68:71]\n"
      "ds_read_b128 v[212:215], %[oa] offset:24832\n"
      "s_waitcnt lgkmcnt(11)\n"
      "v_mfma_f32_16x16x32_bf16 v[64:67], v[172:175], v[216:219], v[64:67]\n"
      "ds_read_b128 v[216:219], %[oa] offset:26624\n"
      "s_waitcnt lgkmcnt(11)\n"
      "v_mfma_f32_16x16x32_bf16 v[68:71], v[172:175], v[220:223], v[68:71]\n"
      "ds_read_b128 v[220:223], %[oa] offset:26880\n"
      "s_waitcnt lgkmcnt(11)\n"
      "v_mfma_f32_16x16x32_bf16 v[64:67], v[176:179], v[224:227], v[64:67]\n"
      "ds_read_b128 v[224:227], %[oa] offset:28672\n"
      "s_waitcnt lgkmcnt(11)\n"
      "v_mfma_f32_16x16x32_bf16 v[68:71], v[176:179], v[228:231], v[68:71]\n"
      "ds_read_b128 v[228:231], %[oa] offset:28928\n"
      "s_waitcnt lgkmcnt(11)\n"
      "v_mfma_f32_16x16x32_bf16 v[64:67], v[180:183], v[232:235], v[64:67]\n"
      "ds_read_b128 v[232:235], %[oa] offset:30720\n"
      "s_waitcnt lgkmcnt(11)\n"
      "v_mfma_f32_16x16x32_bf16 v[68:71], v[180:183], v[236:239], v[68:71]\n"
      "ds_read_b128 v[236:239], %[oa] offset:30976\n"
      "s_waitcnt lgkmcnt(11)\n"
      "v_mfma_f32_16x16x32_bf16 v[64:67], v[184:187], v[240:243], v[64:67]\n"
      "s_waitcnt lgkmcnt(10)\n"
      "v_mfma_f32_16x16x32_bf16 v[68:71], v[184:187], v[244:247], v[68:71]\n"
      "s_waitcnt lgkmcnt(9)\n"
      "v_mfma_f32_16x16x32_bf16 v[64:67], v[188:191], v[248:251], v[64:67]\n"
      "s_waitcnt lgkmcnt(8)\n"
      "v_mfma_f32_16x16x32_bf16 v[68:71], v[188:191], v[252:255], v[68:71]\n"
      "s_waitcnt lgkmcnt(7)\n"
      "v_mfma_f32_16x16x32_bf16 v[64:67], v[192:195], v[208:211], v[64:67]\n"
      "s_waitcnt lgkmcnt(6)\n"
      "v_mfma_f32_16x16x32_bf16 v[68:71], v[192:195], v[212:215], v[68:71]\n"
      "s_waitcnt lgkmcnt(5)\n"
      "v_mfma_f32_16x16x32_bf16 v[64:67], v[196:199], v[216:219], v[64:67]\n"
      "s_waitcnt lgkmcnt(4)\n"
      "v_mfma_f32_16x16x32_bf16 v[68:71], v[196:199], v[220:223], v[68:71]\n"
      "s_waitcnt lgkmcnt(3)\n"
      "v_mfma_f32_16x16x32_bf16 v[64:67], v[200:203], v[224:227], v[64:67]\n"
      "s_waitcnt lgkmcnt(2)\n"
      "v_mfma_f32_16x16x32_bf16 v[68:71], v[200:203], v[228:231], v[68:71]\n"
      "s_waitcnt lgkmcnt(1)\n"
      "v_mfma_f32_16x16x32_bf16 v[64:67], v[204:207], v[232:235], v[64:67]\n"
      "s_waitcnt lgkmcnt(0)\n"
      "v_mfma_f32_16x16x32_bf16 v[68:71], v[204:207], v[236:239], v[68:71]\n"
      "s_nop 7\n"
        : "=&{v[64:67]}"(a0), "=&{v[68:71]}"(a1)
        : "{v[144:159]}"(wof[0]), "{v[160:175]}"(wof[1]), "{v[176:191]}"(wof[2]), "{v[192:207]}"(wof[3]), [oa] "v"(oa_)
        : "memory",
        "v208", "v209", "v210", "v211", "v212", "v213", "v214", "v215", "v216", "v217", "v218", "v219",
        "v220", "v221", "v222", "v223", "v224", "v225", "v226", "v227", "v228", "v229", "v230", "v231",
        "v232", "v233", "v234", "v235", "v236", "v237", "v238", "v239", "v240", "v241", "v242", "v243",
        "v244", "v245", "v246", "v247", "v248", "v249", "v250", "v251", "v252", "v253", "v254", "v255"
    );
#pragma unroll
    for (int tt = 0; tt < 2; ++tt) {
      const f32x4 a = tt ? a1 : a0;
      u32 p0 = pack2(a[0], a[1]), p1 = pack2(a[2], a[3]);
      asm volatile("" : "+v"(p0), "+v"(p1));
      h2[tt] = u32x2{pack2(xs[tt][0] + lo16(p0), xs[tt][1] + hi16(p0)), pack2(xs[tt][2] + lo16(p1), xs[tt][3] + hi16(p1))};
    }
    const u32x4 h4 = join16(h2[0], h2[1]);
    sts128(L, hta, h4);
    f32x4 r4 = {0.f, 0.f, 0.f, t4};
#pragma unroll
    for (int i = 0; i < 4; ++i) {
      r4[0] = dot2(h4[i], ONES, r4[0]);
      r4[1] = dot2(h4[i], h4[i], r4[1]);
      r4[2] = dot2(h4[i], wr4[i], r4[2]);
    }
    *(LAS f32x4*)(L + L_PS + w * 1024 + lane * 16) = r4;
  }
  lds_barrier();
  u32x4 ff[8];
  auto ffn_frags = [&]() {
    const u32 pv = L_FP + w * 2048;
    const u32 q4 = (lane >> 2) & 3, pp = lane & 3;
#pragma unroll
    for (int i = 0; i < 4; ++i) {
      const u32x16& src = i < 2 ? ffa : ffb;
      const int o8 = (i & 1) * 8;
      sts128(L, pv + lane * 16, u32x4{src[o8], src[o8 + 1], src[o8 + 2], src[o8 + 3]});
      sts128(L, pv + 1024 + lane * 16, u32x4{src[o8 + 4], src[o8 + 5], src[o8 + 6], src[o8 + 7]});
      const u32 a = role_gu ? pv + (8 * g + q4) * 64 + pp * 8 : pv + (8 * g + q4) * 32 + pp * 8;
      const u32 d4 = role_gu ? 256 : 128, d2 = role_gu ? 32 : 1024;
      ff[2 * i] = ldtr2(L, a, a + d4);
      ff[2 * i + 1] = ldtr2(L, a + d2, a + d2 + d4);
    }
  };
  if (w == 0) {
    f32x4 t8[8];
#pragma unroll
    for (int i = 0; i < 8; ++i) t8[i] = *(LAS f32x4*)(L + L_PS + i * 1024 + lane * 16);
    __builtin_amdgcn_sched_barrier(0);
    const f32x4 t = ((t8[0] + t8[1]) + (t8[2] + t8[3])) + ((t8[4] + t8[5]) + (t8[6] + t8[7]));
    auto s0 = __builtin_amdgcn_permlane32_swap(asu(t[0]), asu(t[2]), false, false);
    auto s1 = __builtin_amdgcn_permlane32_swap(asu(t[1]), asu(t[3]), false, false);
    __builtin_amdgcn_raw_buffer_store_b128(gran(asu(asf(s0[0]) + asf(s0[1])), asu(asf(s1[0]) + asf(s1[1])), tag), rs,
                                           WS_ST + (panel * 4 + cq) * 1024 + lane * 16, 0, AUX_SC1);
    asm volatile("" ::: "memory");
    if (role_gu | role_dn) {
      ffn_frags();
      __builtin_amdgcn_s_sleep(5);
    } else {
      __builtin_amdgcn_s_sleep(10);
    }
    V4 q4;
    poll(q4,
         [&](V4& d) {
#pragma unroll
           for (int i = 0; i < 4; ++i) d.v[i] = __builtin_amdgcn_raw_buffer_load_b128(rs, WS_ST + (panel * 4 + i) * 1024 + lane * 16, 0, AUX_SC1);
         },
         [&](const V4& d) { return __all(fresh(d.v[0], tag) & fresh(d.v[1], tag) & fresh(d.v[2], tag) & fresh(d.v[3], tag)) != 0; });
    const u32x4* q = q4.v;
    const float u0 = (asf(q[0].x) + asf(q[1].x)) + (asf(q[2].x) + asf(q[3].x)), u1 = (asf(q[0].z) + asf(q[1].z)) + (asf(q[2].z) + asf(q[3].z));
    auto e0 = __builtin_amdgcn_permlane32_swap(asu(u0), asu(u0), false, false);
    auto e1 = __builtin_amdgcn_permlane32_swap(asu(u1), asu(u1), false, false);
    const float a1 = asf(e0[0]), a3 = asf(e0[1]), a2 = asf(e1[0]), a4 = asf(e1[1]);
    const f32x2 sc = ln_scalars(a1, a2);
    const float r = (a3 - sc[0] * a4) * sc[1];
    u32 rb = asu(r);
    rb = (rb & 0x80000000u) ? ~rb : (rb | 0x80000000u);
    const u32 best = half_max(rb);
    const u32 bt = 32 * panel + __builtin_ctzll(__ballot(rb == best));
    if (lane < NREP && cq == 0) __builtin_amdgcn_raw_buffer_store_b128(gran(best, bt, tag), rs, WS_BEST + (lane * 64 + panel) * BSTR, 0, AUX_SC1);
    {
      const u32 bl = bt & 31, hw = *(LAS u32*)(L + L_HT + bl * 272 + lane * 4);
      const u32 nw = ln_pair(hw, asf(__builtin_amdgcn_readlane(asu(sc[0]), bl)), asf(__builtin_amdgcn_readlane(asu(sc[1]), bl)));
      __builtin_amdgcn_raw_buffer_store_b64(u32x2{nw, (lane & 1) ? (u32)(tag >> 32) : (u32)tag}, rs,
                                            WS_ROWN + panel * 2048 + cq * 512 + lane * 8, 0, AUX_SC1);
    }
    if (lane < 32) *(LAS f32x2*)(L + L_TOK + lane * 8) = sc;
    if (lane == 0) *(LAS u32*)(L + L_BT) = bt;
  } else if (role_gu | role_dn) {
    __builtin_amdgcn_s_sleep(10);
    ffn_frags();
  }
  lds_barrier();
  const __amdgpu_buffer_rsrc_t ro = __builtin_amdgcn_make_buffer_rsrc((void*)(((u64)uni((u32)(pout >> 32)) << 32) | uni((u32)pout)), 0, 2048 * DM * 2, 0x00020000);
  const u32 brow = uni(*(LAS u32*)(L + L_BT)) & 31;
  {
    auto store_rows = [&]() {
      const u32 row = 4 * ((w - 1 + (brow >> 2)) & 7) + g;
      const u32x4 h4 = lds128(L, L_HT + row * 272 + l15 * 16);
      const f32x2 sc = *(LAS f32x2*)(L + L_TOK + row * 8);
      u32x4 ow;
#pragma unroll
      for (int i = 0; i < 4; ++i) {
        const u32 n = ln_pair(h4[i], sc[0], sc[1]);
        ow[i] = pack2(lo16(h4[i]) + lo16(n), hi16(h4[i]) + hi16(n));
      }
      __builtin_amdgcn_raw_buffer_store_b128(ow, ro, ((32 * panel + row) * DM + 128 * cq) * 2 + l15 * 16, 0, AUX_SC1);
      if (w == 1) {
        const u32 hw = *(LAS u32*)(L + L_HT + brow * 272 + lane * 4);
        VMWAIT(0);
        __builtin_amdgcn_raw_buffer_store_b64(u32x2{hw, (lane & 1) ? (u32)(tag >> 32) : (u32)tag}, rs,
                                              WS_ROWH + panel * 2048 + cq * 512 + lane * 8, 0, AUX_SC1);
      }
    };
    if (w != 0 || !(role_gu | role_dn)) store_rows();
    if (role_gu | role_dn) {
      if (w == 0) {
        u32 cand = panel, wtok = 0, wkey = 0;
        V3 bv;
        poll(bv,
              [&](V3& d) {
                d.c = cand;
                d.v[0] = __builtin_amdgcn_raw_buffer_load_b128(rs, WS_BEST + ((bid & (NREP - 1)) * 64 + lane) * BSTR, 0, AUX_SC1);
                d.v[1] = __builtin_amdgcn_raw_buffer_load_b128(rs, WS_ROWN + cand * 2048 + lane * 16, 0, AUX_SC1);
                d.v[2] = __builtin_amdgcn_raw_buffer_load_b128(rs, WS_ROWN + cand * 2048 + 1024 + lane * 16, 0, AUX_SC1);
              },
              [&](const V3& d) {
                const int here = fresh(d.v[0], tag);
                const u32 key = here ? d.v[0].x : 0u;
                wkey = wave_max(key);
                cand = __builtin_ctzll(__ballot(key == wkey));
                wtok = __builtin_amdgcn_readlane(d.v[0].z, cand) & 2047u;
                return __all(here & fresh(d.v[1], tag) & fresh(d.v[2], tag)) && cand == d.c;
              });
        const u32x4 r0 = bv.v[1], r1 = bv.v[2];
        *(LAS u32x2*)(L + L_ROW + lane * 8) = u32x2{r0.x, r0.z};
        *(LAS u32x2*)(L + L_ROW + 512 + lane * 8) = u32x2{r1.x, r1.z};
        if (lane == 0) *(LAS u32x2*)(L + L_WIN) = u32x2{wtok, wkey};
      }
      if (role_gu) {
        asm volatile("; gate/up" ::: "memory");
        lds_barrier();
        const u32x4 hn0 = lds128(L, L_ROW + w * 128 + g * 16), hn1 = lds128(L, L_ROW + w * 128 + 64 + g * 16);
        f32x4 d[4];
#pragma unroll
        for (int rt = 0; rt < 4; ++rt) d[rt] = mfma16(ff[4 * (rt >> 1) + (rt & 1)], hn0, f32x4{0.f, 0.f, 0.f, 0.f});
#pragma unroll
        for (int rt = 0; rt < 4; ++rt) d[rt] = mfma16(ff[4 * (rt >> 1) + 2 + (rt & 1)], hn1, d[rt]);
        const f32x4 ds = (l15 & 2) ? ((l15 & 1) ? d[3] : d[2]) : ((l15 & 1) ? d[1] : d[0]);
        *(LAS f32x4*)(L + L_RED + w * 256 + (l15 & 3) * 64 + g * 16) = ds;
        lds_barrier();
        if (w == 0) {
          float u[8];
#pragma unroll
          for (int i = 0; i < 8; ++i) u[i] = *(LAS float*)(L + L_RED + i * 256 + lane * 4);
          const float v = rbf(((u[0] + u[1]) + (u[2] + u[3])) + ((u[4] + u[5]) + (u[6] + u[7])));
          auto r = __builtin_amdgcn_permlane32_swap(asu(v), asu(v), false, false);
          const float gv = asf(r[0]), up = asf(r[1]);
          const float gt = rbf(gv * __builtin_amdgcn_rcpf(1.f + __expf(-gv)));
          const float a = rbf(gt * up);
          const float an = asf(__builtin_amdgcn_update_dpp(0, (int)asu(a), 0xF5, 0xF, 0xF, true));
          const u32 pk = pack2(a, an), pn = (u32)__builtin_amdgcn_update_dpp(0, (int)pk, 0x102, 0xF, 0xF, true);
          if (lane < 32 && !(lane & 3)) __builtin_amdgcn_raw_buffer_store_b128(gran(pk, pn, tag), rs, WS_ACT + ug * 128 + lane * 4, 0, AUX_SC1);
          store_rows();
          if (ug == 0 && lane == 0) __hip_atomic_store((GAS u64*)(pws + WS_EPOCH), tag, __ATOMIC_RELAXED, __HIP_MEMORY_SCOPE_AGENT);
        }
      } else {
        asm volatile("; down" ::: "memory");
        lds_barrier();
        if (w == 0) store_rows();
        const u32x2 win = *(LAS u32x2*)(L + L_WIN);
        const u32 wtok = win.x, wkey = win.y;
        const u32 off = lane < 56 ? WS_ACT + w * 896 + lane * 16 : WS_ROWH + (wtok >> 5) * 2048 + dc * 4 + (lane & 3) * 16;
        const float r = rbf(asf((wkey & 0x80000000u) ? (wkey & 0x7FFFFFFFu) : ~wkey));
        float sg = rbf(__builtin_amdgcn_rcpf(1.f + __expf(-r)));
        const u32 nq = *(LAS u32*)(L + L_ROW + dc * 2 + (l15 >> 1) * 4);
        float nval = (lane & 1) ? hi16(nq) : lo16(nq);
        u32 ooff = (wtok * DM + dc + lane) * 2;
        asm volatile("" : "+v"(sg), "+v"(nval), "+v"(ooff));
        u32x4 a4;
        if (w != 0) __builtin_amdgcn_s_sleep(4);
        __builtin_amdgcn_s_sleep(18);
        poll(a4, [&](u32x4& d) { d = __builtin_amdgcn_raw_buffer_load_b128(rs, off, 0, AUX_SC1); },
             [&](const u32x4& d) { return __all(fresh(d, tag)) != 0; });
        *(LAS u32x2*)(L + L_FP + w * 2048 + lane * 8) = u32x2{a4.x, a4.z};
        u32x4 ab[7];
#pragma unroll
        for (int i = 0; i < 7; ++i) ab[i] = lds128(L, L_FP + w * 2048 + i * 64 + g * 16);
        const u32 hq = *(LAS u32*)(L + L_FP + w * 2048 + 448 + (l15 >> 1) * 4);
        __builtin_amdgcn_sched_barrier(0);
        f32x4 d = {0.f, 0.f, 0.f, 0.f};
#pragma unroll
        for (int i = 0; i < 7; ++i) d = mfma16(ff[i], ab[i], d);
        *(LAS f32x4*)(L + L_RED + w * 64 + g * 16) = d;
        lds_barrier();
        if (w == 0) {
          float v = *(LAS float*)(L + L_RED + lane * 4) + *(LAS float*)(L + L_RED + 256 + lane * 4);
          v = rbf(add32(add16(v)));
          const float res = ((lane & 1) ? hi16(hq) : lo16(hq)) + rbf(nval + rbf(v * sg));
          const float rn = asf(__builtin_amdgcn_update_dpp(0, (int)asu(res), 0xF5, 0xF, 0xF, true));
          if (lane < 16 && !(lane & 1)) __builtin_amdgcn_raw_buffer_store_b32(pack2(res, rn), ro, ooff, 0, AUX_SC1);
        }
      }
    }
  }
}

}

extern "C" void kernel_launch(void* const* d_in, const int* in_sizes, int n_in, void* d_out, int out_size, void* d_ws,
                              size_t ws_size, hipStream_t stream) {
  (void)in_sizes; (void)n_in; (void)out_size; (void)ws_size;
  const u32 fmt = 0x00020000u;
  Params p;
  p.d[D_X] = {d_in[0], 2048 * DM * 2, fmt};
  p.d[D_WQKV] = {d_in[1], DM * 3 * DM * 2, fmt};
  p.d[D_WS] = {d_ws, WS_BYTES, fmt};
  p.d[D_WO] = {d_in[2], DM * DM * 2, fmt};
  p.d[D_WG] = {d_in[5], DM * FF * 2, fmt};
  p.d[D_WU] = {d_in[6], DM * FF * 2, fmt};
  p.d[D_WD] = {d_in[7], DM * FF * 2, fmt};
  p.d[D_NULL] = {d_ws, 0, fmt};
  p.d[D_WR] = {d_in[4], DM * 2, fmt};
  p.d[D_OUT] = {d_out, 2048 * DM * 2, fmt};
  (void)hipFuncSetAttribute((const void*)fused_block_kernel, hipFuncAttributeMaxDynamicSharedMemorySize, L_BYTES);
  hipLaunchKernelGGL(fused_block_kernel, dim3(NWG), dim3(NT), L_BYTES, stream, p);
}
```
